```python
import torch

def _softmax(x, axis=-1):
    return torch.softmax(x, dim=axis)

def _gelu(x):
    return torch.nn.functional.gelu(x, approximate='tanh')

def _silu(x):
    return torch.nn.functional.silu(x)

def _sigmoid(x):
    return torch.sigmoid(x)

def _layernorm(x, axis=-1, eps=1e-05):
    m = x.mean(dim=axis, keepdim=True)
    v = x.var(dim=axis, keepdim=True, unbiased=False)
    return (x - m) / torch.sqrt(v + eps)

def _scatter_add(tgt, idx, src, axis=0):
    return tgt.clone().index_add_(axis, idx, src)

def _segment_sum(src, seg, n):
    out = torch.zeros((n,) + src.shape[1:], dtype=src.dtype)
    return out.index_add_(0, seg, src)

def _pad(x, pw):
    flat = tuple((v for pair in reversed(pw) for v in pair))
    return torch.nn.functional.pad(x, flat)
kernel_name = 'adaLN_cond__memory_aug_recurrent_mem_tr__mixer_gmlp__prompt'

def setup_inputs():

    _w = lambda a: (lambda t: t.to(torch.bfloat16) if t.is_floating_point() else t)(torch.as_tensor(a))

    def _A_setup():
        B, S, Dm = (1, 2048, 512)
        H, Dh = (32, 128)
        M, seg = (8, 32)
        seg = min(seg, S)
        S = S // seg * seg
        return {'x': _w((lambda *s: torch.randn(*s, dtype=torch.bfloat16) * 0.05)(B, S, Dm)), 'mem0': _w((lambda *s: torch.randn(*s, dtype=torch.bfloat16) * 0.4)(B, M, Dm)), 'Wq': _w((lambda *s: torch.randn(*s, dtype=torch.bfloat16) * 3.2)(Dm, H, Dh) * Dm ** (-0.5)), 'Wk': _w((lambda *s: torch.randn(*s, dtype=torch.bfloat16) * 3.2)(Dm, H, Dh) * Dm ** (-0.5)), 'Wv': _w((lambda *s: torch.randn(*s, dtype=torch.bfloat16) * 0.4)(Dm, H, Dh) * Dm ** (-0.5)), 'Wo': _w((lambda *s: torch.randn(*s, dtype=torch.bfloat16) * 0.4)(H, Dh, Dm) * (H * Dh) ** (-0.5)), 'seg_len': seg}
    _A_in = _A_setup()

    def _B_setup():
        B, S, D, F = (1, 2048, 512, 1376)
        Fh = F // 2
        return {'x': _w((lambda *s: torch.randn(*s, dtype=torch.bfloat16) * 0.05)(B, S, D)), 'w_in': _w((lambda *s: torch.randn(*s, dtype=torch.bfloat16) * 0.8)(D, 2 * Fh)), 'w_sp': _w(0.01 * (lambda *s: torch.randn(*s, dtype=torch.bfloat16) * 0.8)(S, S)), 'b_sp': _w(1.0 + 0.01 * (lambda *s: torch.randn(*s, dtype=torch.bfloat16) * 0.05)(S)), 'w_out': _w((lambda *s: torch.randn(*s, dtype=torch.bfloat16) * 0.05)(Fh, D))}
    _B_in = _B_setup()
    return {'A_x': _A_in['x'], 'A_mem0': _A_in['mem0'], 'A_Wq': _A_in['Wq'], 'A_Wk': _A_in['Wk'], 'A_Wv': _A_in['Wv'], 'A_Wo': _A_in['Wo'], 'A_seg_len': _A_in['seg_len'], 'B_x': _B_in['x'], 'B_w_in': _B_in['w_in'], 'B_w_sp': _B_in['w_sp'], 'B_b_sp': _B_in['b_sp'], 'B_w_out': _B_in['w_out'], 'c': _w((lambda *s: torch.randn(*s, dtype=torch.bfloat16) * 0.1)(1, 512) * 8.0), 'w_mod': _w((lambda *s: torch.randn(*s, dtype=torch.bfloat16) * 0.1)(512, 6 * 512) * 512 ** (-0.5) * 8.0)}

def reference(A_x, A_mem0, A_Wq, A_Wk, A_Wv, A_Wo, A_seg_len, B_x, B_w_in, B_w_sp, B_b_sp, B_w_out, c, w_mod):

    def _A_ref(x, mem0, Wq, Wk, Wv, Wo, seg_len):
        B, S, Dm = x.shape
        H, Dh = (Wq.shape[1], Wq.shape[2])
        M = mem0.shape[1]
        scale = Dh ** (-0.5)
        n_seg = S // seg_len
        L = M + seg_len
        idx = torch.arange(L)
        mask = (idx[:, None] < M) | (idx[:, None] >= idx[None, :])
        mem = mem0
        outs = []
        for s in range(n_seg):
            xs = x[:, s * seg_len:(s + 1) * seg_len, :].clone()
            h = torch.concatenate([mem, xs], axis=1)
            q = torch.einsum('btd,dhe->bhte', h, Wq)
            k = torch.einsum('btd,dhe->bhte', h, Wk)
            v = torch.einsum('btd,dhe->bhte', h, Wv)
            scores = torch.einsum('bhqe,bhke->bhqk', q, k) * scale
            scores = torch.where(mask[None, None], scores, -1000000000.0)
            p = _softmax(scores, axis=-1)
            o = torch.einsum('bhqk,bhke->bhqe', p, v)
            o = torch.einsum('bhte,hed->btd', o, Wo)
            mem = o[:, :M, :].clone()
            outs.append(o[:, M:, :])
        return torch.concatenate(outs, axis=1)

    def _B_ref(x, w_in, w_sp, b_sp, w_out):
        B, S, D = x.shape
        F = w_in.shape[-1]
        Fh = F // 2
        z = _gelu(torch.einsum('bsd,df->bsf', x, w_in))
        z1 = z[..., :Fh].clone()
        z2 = _layernorm(z[..., Fh:], axis=-1)
        g = torch.einsum('ts,bsf->btf', w_sp, z2) + b_sp[None, :, None]
        return torch.einsum('bsf,fd->bsd', z1 * g, w_out)
    x = A_x
    mod = (_silu(c) @ w_mod).reshape(c.shape[0], 6, 1, -1)
    sa, ca, ga, sb, cb, gb = (mod[:, 0].clone(), mod[:, 1].clone(), mod[:, 2].clone(), mod[:, 3].clone(), mod[:, 4].clone(), mod[:, 5].clone())
    h = _layernorm(x, axis=-1) * (1 + ca) + sa
    x = x + ga * _A_ref(h, A_mem0, A_Wq, A_Wk, A_Wv, A_Wo, A_seg_len)
    h = _layernorm(x, axis=-1) * (1 + cb) + sb
    x = x + gb * _B_ref(h, B_w_in, B_w_sp, B_b_sp, B_w_out)
    return x
```

```cpp
#include <hip/hip_runtime.h>
#include <stdint.h>
typedef unsigned short u16;
typedef unsigned v4u __attribute__((ext_vector_type(4)));
typedef unsigned v2u __attribute__((ext_vector_type(2)));
typedef __bf16 bf16x8 __attribute__((ext_vector_type(8)));
typedef __bf16 bf16x2 __attribute__((ext_vector_type(2)));
typedef short s16x4 __attribute__((ext_vector_type(4)));
typedef float f32x4 __attribute__((ext_vector_type(4)));
typedef float f32x2 __attribute__((ext_vector_type(2)));
typedef __amdgpu_buffer_rsrc_t rsrc_t;
#define DEVI __device__ __forceinline__
#define UNR _Pragma("unroll")
#define SB() __builtin_amdgcn_sched_barrier(0)
constexpr unsigned SENT = 0xFFFFFFFFu, OOB = 0x80000000u;
constexpr size_t MB = 1u << 20;
constexpr size_t O_RING = 0, O_FLG = 0x180000, O_MOD = 0x1C0000, O_KV = 18 * MB, O_QF = 50 * MB, O_KM = 66 * MB, O_P2 = 82 * MB, O_HF = 146 * MB, O_X1 = 148 * MB,
                 O_Z1 = 150 * MB, O_Z2 = 153 * MB, O_UF = 156 * MB, O_WOUT = 161 * MB, O_ST = 162 * MB, O_DBG = 163 * MB, O_WS = 168 * MB;
constexpr int RING_BYTES = 69 * 16384, MBASE = 64 * 16384, SYNCO = 68 * 16384, CALO = 69 * 16384;
constexpr int F_REC = 128, F_QKV = 256, F_H = 384, F_ATT = 512, F_RED = 640, F_WIN = 672, F_WS = 704, F_N = 768;
#define XK 3
#define SK 5
#define FW 0
#ifndef NV
#define NV 4
#endif
#define P_D1 16
#define P_D2 43
#define P_MG 10
#define P_D0 116
DEVI rsrc_t mkr(const void* p, unsigned bytes) { return __builtin_amdgcn_make_buffer_rsrc((void*)p, 0, bytes, 0x00020000); }
DEVI v4u ld16c(rsrc_t r, unsigned v, unsigned s) { return __builtin_amdgcn_raw_buffer_load_b128(r, v, s, 16); }
DEVI v2u ld8c(rsrc_t r, unsigned v, unsigned s) { return __builtin_amdgcn_raw_buffer_load_b64(r, v, s, 16); }
DEVI unsigned ld4c(rsrc_t r, unsigned v, unsigned s) { return __builtin_amdgcn_raw_buffer_load_b32(r, v, s, 16); }
DEVI v4u ld16(rsrc_t r, unsigned v, unsigned s) { return __builtin_amdgcn_raw_buffer_load_b128(r, v, s, 0); }
DEVI void st16c(rsrc_t r, unsigned v, unsigned s, v4u x) { __builtin_amdgcn_raw_buffer_store_b128(x, r, v, s, 16); }
DEVI void st8c(rsrc_t r, unsigned v, unsigned s, v2u x) { __builtin_amdgcn_raw_buffer_store_b64(x, r, v, s, 16); }
DEVI void st4c(rsrc_t r, unsigned v, unsigned s, unsigned x) { __builtin_amdgcn_raw_buffer_store_b32(x, r, v, s, 16); }
DEVI float f_(unsigned u) { return __uint_as_float(u); }
DEVI unsigned u_(float f) { return __float_as_uint(f); }
DEVI unsigned pk(float a, float b) { f32x2 t = {a, b}; return __builtin_bit_cast(unsigned, __builtin_convertvector(t, bf16x2)); }
DEVI float lo(unsigned u) { return f_(u << 16); }
DEVI float hi(unsigned u) { return f_(u & 0xffff0000u); }
DEVI float rb(float a) { return lo(pk(a, a)); }
DEVI v4u pk8(f32x4 a, f32x4 b) { return (v4u){pk(a[0], a[1]), pk(a[2], a[3]), pk(b[0], b[1]), pk(b[2], b[3])}; }
DEVI f32x4 mf32(v4u a, v4u b, f32x4 c) { return __builtin_amdgcn_mfma_f32_16x16x32_bf16(__builtin_bit_cast(bf16x8, a), __builtin_bit_cast(bf16x8, b), c, 0, 0, 0); }
DEVI f32x4 mf16(v2u a, v2u b, f32x4 c) { return __builtin_amdgcn_mfma_f32_16x16x16bf16_1k(__builtin_bit_cast(s16x4, a), __builtin_bit_cast(s16x4, b), c, 0, 0, 0); }
DEVI float gsum(float m) { auto a = __builtin_amdgcn_permlane16_swap(u_(m), u_(m), false, false); m = f_(a[0]) + f_(a[1]); auto b = __builtin_amdgcn_permlane32_swap(u_(m), u_(m), false, false); return f_(b[0]) + f_(b[1]); }
DEVI float gmax(float m) { auto a = __builtin_amdgcn_permlane16_swap(u_(m), u_(m), false, false); m = fmaxf(f_(a[0]), f_(a[1])); auto b = __builtin_amdgcn_permlane32_swap(u_(m), u_(m), false, false); return fmaxf(f_(b[0]), f_(b[1])); }
DEVI float dot2(unsigned a, unsigned b, float c) { return __builtin_amdgcn_fdot2_f32_bf16(__builtin_bit_cast(bf16x2, a), __builtin_bit_cast(bf16x2, b), c, false); }
DEVI void drain() { asm volatile("s_waitcnt vmcnt(0)" ::: "memory"); }
DEVI void bar() { asm volatile("s_waitcnt lgkmcnt(0)\n\ts_barrier" ::: "memory"); }
DEVI unsigned rt() { unsigned long long t; asm volatile("s_memrealtime %0\n\ts_waitcnt lgkmcnt(0)" : "=s"(t) :: "memory"); return (unsigned)t; }
DEVI unsigned ct() { unsigned long long t; asm volatile("s_memtime %0\n\ts_waitcnt lgkmcnt(0)" : "=s"(t) :: "memory"); return (unsigned)t; }
DEVI void tcoarse(unsigned T) { int d = __builtin_amdgcn_readfirstlane(min(max((int)(T - rt()), 0), 150) * 55 >> 6); asm volatile(".p2align 3\nL%=: s_sub_u32 %0, %0, 1\n s_cbranch_scc0 L%=" : "+s"(d) :: "scc"); }
DEVI v4u mkd(const void* q, unsigned bytes) { unsigned long long a = (unsigned long long)q; return (v4u){(unsigned)a, (unsigned)(a >> 32), bytes, 0x00020000u}; }
DEVI void dma(v4u r, unsigned v, unsigned s, unsigned l, int o) { asm volatile("s_mov_b32 m0, %3\n\ts_nop 0\n\tbuffer_load_dwordx4 %0, %1, %2 offen offset:%4 sc1 lds" :: "v"(v), "s"(r), "s"(s), "s"(l), "n"(o) : "memory"); }
DEVI void touch(v4u r, unsigned v, unsigned s, unsigned l) { asm volatile("s_mov_b32 m0, %3\n\ts_nop 0\n\tbuffer_load_dword %0, %1, %2 offen lds" :: "v"(v), "s"(r), "s"(s), "s"(l) : "memory"); }
DEVI int prow(int n, int f) { return 8 * (n >> 2) + (n & 3) + 4 * f; }
DEVI void seen(unsigned v, unsigned voff, int& known) { v = voff == OOB ? 64u : v;
    v = min(v, (unsigned)__builtin_amdgcn_update_dpp(-1, v, 0x128, 15, 15, false)); v = min(v, (unsigned)__builtin_amdgcn_update_dpp(-1, v, 0x124, 15, 15, false));
    v = min(v, (unsigned)__builtin_amdgcn_update_dpp(-1, v, 0x122, 15, 15, false)); v = min(v, (unsigned)__builtin_amdgcn_update_dpp(-1, v, 0x121, 15, 15, false));
    { auto a = __builtin_amdgcn_permlane16_swap(v, v, false, false); v = min(a[0], a[1]); auto b = __builtin_amdgcn_permlane32_swap(v, v, false, false); v = min(min(b[0], b[1]), 64u); }
    known = max(known, (int)__builtin_amdgcn_readfirstlane(v));
    asm volatile("" ::: "memory"); }
DEVI unsigned peek(rsrc_t rF, unsigned voff) { asm volatile("" ::: "memory"); return ld4c(rF, voff, 0); }
DEVI void waitc(rsrc_t rF, unsigned voff, int need, int& known, int& bud) {
    while (known < need) {
        seen(peek(rF, voff), voff, known);
        if (known < need) { __builtin_amdgcn_s_sleep(4); if (--bud < 0) known = 1000; }
    }
    asm volatile("" ::: "memory");
}
struct Par { const u16 *x, *mem0, *Wq, *Wk, *Wv, *Wo, *win, *wsp, *bsp, *wout, *c, *wmod; char* ws; u16* out; int d1, d2, mg, d0, e0, e1; };
struct Pol { unsigned a, b, c; };
#define KEEP(t) asm volatile("" :: "v"(t.a), "v"(t.b), "v"(t.c));
DEVI void waitf(const Par& p, unsigned voff, int& known, Pol& t) {
    if (known >= 64) return;
    int n = 100000;
    asm volatile("buffer_load_dword %0, %4, %6, 0 offen sc1\n s_sleep 7\n buffer_load_dword %1, %4, %6, 0 offen sc1\n s_sleep 7\n buffer_load_dword %2, %4, %6, 0 offen sc1\n"
        "P%=: s_waitcnt vmcnt(2)\n v_cmp_gt_u32 vcc, %5, %0\n s_nop 1\n s_cbranch_vccz E%=\n buffer_load_dword %0, %4, %6, 0 offen sc1\n"
        "s_waitcnt vmcnt(2)\n v_cmp_gt_u32 vcc, %5, %1\n s_nop 1\n s_cbranch_vccz E%=\n buffer_load_dword %1, %4, %6, 0 offen sc1\n"
        "s_waitcnt vmcnt(2)\n v_cmp_gt_u32 vcc, %5, %2\n s_nop 1\n s_cbranch_vccz E%=\n buffer_load_dword %2, %4, %6, 0 offen sc1\n"
        "s_sub_u32 %3, %3, 1\n s_cbranch_scc0 P%=\n E%=:"
        : "+&v"(t.a), "+&v"(t.b), "+&v"(t.c), "+s"(n) : "v"(voff), "v"(voff == OOB ? 0u : 64u), "s"(mkd(p.ws + O_FLG, F_N * 128)) : "vcc", "scc", "memory");
    known = 64;
}
#define TS0
#define TS(i)
#define TSE(k)
#define TA(i) TS(i)
#define TW(i)
#define STAMP(k)

DEVI Par fresh() { auto q = (__attribute__((address_space(4))) const Par*)__builtin_amdgcn_kernarg_segment_ptr(); asm volatile("" : "+s"(q));
    return Par{q->x, q->mem0, q->Wq, q->Wk, q->Wv, q->Wo, q->win, q->wsp, q->bsp, q->wout, q->c, q->wmod, q->ws, q->out, q->d1, q->d2, q->mg, q->d0, q->e0, q->e1}; }
DEVI v4u code_span() { unsigned lo_, hi_, n; asm volatile("s_getpc_b64 s[20:21]\n\ts_add_u32 %0, s20, _Z6k_main3Par@rel32@lo+4\n\ts_addc_u32 %1, s21, _Z6k_main3Par@rel32@hi+12\n\ts_add_u32 %2, s20, _Z5k_out3Par@rel32@lo+20\n\ts_sub_u32 %2, %2, %0" : "=&s"(lo_), "=&s"(hi_), "=&s"(n) :: "s20", "s21", "scc");
    return (v4u){lo_, hi_, n < 0x40000u ? n + 3072 : 0u, 0x00020000u}; }
__global__ __launch_bounds__(256) void k_pre(Par p) {
    __shared__ float red[4][32];
    int b = blockIdx.x; const int tid = threadIdx.x; v4u R[8];
    if (b < 96) {
        const int cg = tid & 3, kr = tid >> 2; float a[8] = {0, 0, 0, 0, 0, 0, 0, 0};
        UNR for (int i = 0; i < 8; ++i) R[i] = *(const v4u*)(p.wmod + (kr + 64 * i) * 3072 + b * 32 + cg * 8);
        UNR for (int i = 0; i < 8; ++i) { float x = lo(p.c[kr + 64 * i]); float s = rb(x / (1.f + __expf(-x)));
            UNR for (int q = 0; q < 4; ++q) { a[2 * q] += s * lo(R[i][q]); a[2 * q + 1] += s * hi(R[i][q]); } }
        UNR for (int q = 0; q < 8; ++q) { float v = a[q]; v += __shfl_xor(v, 4); v += __shfl_xor(v, 8); v = gsum(v); if ((tid & 63) < 4) red[tid >> 6][cg * 8 + q] = v; }
        __syncthreads();
        if (tid < 32) ((u16*)(p.ws + O_MOD))[b * 32 + tid] = (u16)pk(red[0][tid] + red[1][tid] + red[2][tid] + red[3][tid], 0.f);
        return;
    }
    b -= 96;
    if (b < 70) {
        UNR for (int k = 0; k < 4; ++k) { const int q = k * 4096 + tid * 16; v4u v = {SENT, SENT, SENT, SENT};
            if (b == 64) { const int j = (q >> 8) & 31, n = (q >> 4) & 15; v = *(const v4u*)(p.mem0 + (n & 7) * 512 + j * 16 + (n >> 3) * 8); v.x = min(v.x, SENT - 1); v.z = min(v.z, SENT - 1); }
            *(v4u*)(p.ws + O_RING + b * 16384 + q) = v; }
        return;
    }
    b -= 70;
    if (b >= 24) { if (!(p.e1 & 256)) touch(code_span(), tid * 128 + (b - 24) * 32768, 0, (unsigned)(size_t)&red[0][0]); drain(); return; }
    *(v4u*)(p.ws + O_FLG + (b * 256 + tid) * 16) = (v4u){0, 0, 0, 0};
}
constexpr int PRE_GRID = 96 + 70 + 24 + 4;

template <int NF> DEVI void ln_mod(v4u (&x)[NF][4], const v4u (&c1)[4], const v4u (&sh)[4], f32x2* red, int w, int lane) {
    UNR for (int f = 0; f < NF; ++f) { float a = 0, q = 0;
        UNR for (int i = 0; i < 16; ++i) { float l = lo(x[f][i >> 2][i & 3]), h = hi(x[f][i >> 2][i & 3]); a += l + h; q += l * l; q += h * h; }
        a = gsum(a); q = gsum(q); if (lane < 16) red[(f * 4 + w) * 16 + lane] = (f32x2){a, q}; }
    bar();
    UNR for (int f = 0; f < NF; ++f) { f32x2 s = red[f * 64 + (lane & 15)] + red[f * 64 + 16 + (lane & 15)] + red[f * 64 + 32 + (lane & 15)] + red[f * 64 + 48 + (lane & 15)];
        const float mean = s[0] * (1.f / 512), var = fmaxf(s[1] * (1.f / 512) - mean * mean, 0.f), m = rb(mean), in = 1.f / rb(sqrtf(rb(rb(var) + 1e-5f)));
        UNR for (int i = 0; i < 16; ++i) { unsigned t = x[f][i >> 2][i & 3], cc = c1[i >> 2][i & 3], ss = sh[i >> 2][i & 3];
            t = pk(lo(t) - m, hi(t) - m); t = pk(lo(t) * in, hi(t) * in); t = pk(lo(t) * lo(cc), hi(t) * hi(cc)); x[f][i >> 2][i & 3] = pk(lo(t) + lo(ss), hi(t) + hi(ss)); } }
}
DEVI v4u plus1(v4u c) { return (v4u){pk(1.f + lo(c.x), 1.f + hi(c.x)), pk(1.f + lo(c.y), 1.f + hi(c.y)), pk(1.f + lo(c.z), 1.f + hi(c.z)), pk(1.f + lo(c.w), 1.f + hi(c.w))}; }
template <int NF> DEVI void ln_roll(v4u (&x)[NF][4], const v4u (&c1)[4], const v4u (&sh)[4], f32x2* red, int w, int lane) {
    _Pragma("unroll 1") for (int f = 0; f < NF; ++f) { v4u t[1][4] = {{x[0][0], x[0][1], x[0][2], x[0][3]}};
        ln_mod<1>(t, c1, sh, red + f * 64, w, lane);
        UNR for (int i = 0; i < 4; ++i) { UNR for (int k = 1; k < NF; ++k) x[k - 1][i] = x[k][i]; x[NF - 1][i] = t[0][i]; } }
}
DEVI void ln1(const Par& p, char* lds, int H, int w, int lane, rsrc_t rF) {
    const int n = lane & 15, g = lane >> 4; const u16* mod = (const u16*)(p.ws + O_MOD); v4u x[2][4], c1[4], sh[4];
    UNR for (int i = 0; i < 4; ++i) { const int col = 128 * w + 64 * (i >> 1) + 16 * g + 8 * (i & 1);
        UNR for (int f = 0; f < 2; ++f) x[f][i] = *(const v4u*)(p.x + (H * 32 + prow(n, f)) * 512 + col);
        sh[i] = *(const v4u*)(mod + col); c1[i] = plus1(*(const v4u*)(mod + 512 + col)); }
    ln_roll<2>(x, c1, sh, (f32x2*)lds, w, lane);
    rsrc_t rH = mkr(p.ws + O_HF, 2 * MB);
    UNR for (int i = 0; i < 8; ++i) st16c(rH, lane * 16 + (i & 3) * 1024, (H * 32 + (i & 4) * 4 + 4 * w) * 1024, x[i >> 2][i & 3]);
    drain(); bar();
    if (w == 0) st4c(rF, 0, (F_H + H) * 128, 64);
}
template <int PITCH, int HALF = 128> DEVI void wdma(const void* src, unsigned bytes, unsigned so, unsigned l, int lane) { const v4u d = mkd(src, bytes); const unsigned ch = (lane & 15) ^ (4 * (lane >> 4)), vo = (lane >> 4) * PITCH + (ch >> 3) * HALF + (ch & 7) * 16;
    UNR for (int q = 0; q < 32; ++q) asm volatile("s_mov_b32 m0, %1\n\ts_add_u32 %1, %1, 1024\n\tbuffer_load_dwordx4 %2, %3, %0 offen lds\n\ts_add_u32 %0, %0, %4" : "+s"(so), "+s"(l) : "v"(vo), "s"(d), "n"(4 * PITCH) : "memory", "scc"); }
DEVI v4u trd(const char* l) { typedef __attribute__((address_space(3))) s16x4* T;
    const v2u a = __builtin_bit_cast(v2u, __builtin_amdgcn_ds_read_tr16_b64_v4i16((T)l)), b = __builtin_bit_cast(v2u, __builtin_amdgcn_ds_read_tr16_b64_v4i16((T)(l + 1024))); return (v4u){a.x, a.y, b.x, b.y}; }
DEVI void wotile(const Par& p, char* lds, int u, int w, int lane) {
    const int n = lane & 15, g = lane >> 4;
    wdma<1024>(p.wout, 688 * 1024, u * 131072 + w * 256, (unsigned)(size_t)lds + w * 32768, lane); drain();
    UNR for (int t = 0; t < 4; ++t) if (4 * u + t < 22) UNR for (int q = 0; q < 8; ++q) { const v4u v = trd(lds + w * 32768 + (8 * g + (n >> 2)) * 256 + ((q >> 1) ^ (n >> 2)) * 64 + (n & 3) * 16 + t * 8192 + 8 * (q & 1));
        *(v4u*)(p.ws + O_WOUT + ((w * 8 + q) * 22 + 4 * u + t) * 1024 + lane * 16) = 4 * u + t == 21 && g >= 2 ? (v4u){0, 0, 0, 0} : v; }
}
template <bool ISV> DEVI void qkv(const Par& p, char* lds, const v4u (&W)[16][2], int mat, int h, int w, int lane, rsrc_t rF, int& bud) {
    rsrc_t rH = mkr(p.ws + O_HF, 2 * MB), rO = mkr(p.ws + (mat ? O_KV : O_QF), 32 * MB);
    const unsigned ob = mat ? (h * 256 + w) * 4096 + (mat - 1) * 2048 : (h * 256 + w) * 2048, os = mat ? 14 : 13;
    int k0 = 0; waitc(rF, lane < 4 ? (F_H + lane) * 128 : OOB, 64, k0, bud); STAMP(1)
    v4u nx[16];
    UNR for (int i = 0; i < 16; ++i) nx[i] = ld16c(rH, lane * 16 + (i & 3) * 1024, (w * 16 + (i & 12)) * 1024);
    bar();
    UNR for (int i = 0; i < 16; ++i) *(v4u*)(lds + (w * 16 + i) * 1024 + lane * 16) = nx[i];
    bar();
    TS0
    for (int s = 0; s < 32; ++s) {
        const char* cur = lds + (s & 1) * 65536 + lane * 16; f32x4 a[2][2][2] = {}; v4u hf[64];
        UNR for (int i = 0; i < 6; ++i) hf[i] = *(const v4u*)(cur + ((i & 1) * 16 + (i >> 1)) * 1024);
        UNR for (int i = 0; i < 16; ++i) nx[i] = ld16c(rH, lane * 16 + (i & 3) * 1024, (((s + 1) & 31) * 64 + w * 16 + (i & 12)) * 1024);
        SB();
        UNR for (int i = 0; i < 64; ++i) { const int k = i + 6, f = i & 1, P = (i >> 1) & 15, e = i >> 5;
            if (k < 64) hf[k] = *(const v4u*)(cur + ((k >> 5) * 32 + (k & 1) * 16 + ((k >> 1) & 15)) * 1024);
            UNR for (int u = 0; u < 2; ++u) { if (ISV) a[e][u][f] = mf32(hf[i], W[P][u], a[e][u][f]); else a[e][f][u] = mf32(W[P][u], hf[i], a[e][f][u]); }
            if (i == 34 || i == 36) st16c(rO, lane * 16 + (i - 34) * 512, (((2 * s + h) & 63u) << os) + ob, pk8(a[0][(i - 34) / 2][0], a[0][(i - 34) / 2][1]));
            if (i == 47) { TS(0) drain(); TS(1) }
            if (i >= 48) *(v4u*)(lds + ((s + 1) & 1) * 65536 + (w * 16 + i - 48) * 1024 + lane * 16) = nx[i - 48];
            SB(); }
        UNR for (int f = 0; f < 2; ++f) st16c(rO, lane * 16 + f * 1024, (((2 * s + 1 + h) & 63u) << os) + ob, pk8(a[1][f][0], a[1][f][1]));
        if (s == 31 || s == 0) drain();
        TS(2) bar(); TS(3)
        if (w == 0) st4c(rF, 0, (F_QKV + mat * 32 + h) * 128, s == 31 ? 64 : s ? 2 * s + 1 : 2);
        if (s == 0) { STAMP(2) k0 = 0; waitc(rF, (F_H + lane) * 128, 64, k0, bud); }
    }
    TSE(0)
}
DEVI void warm(const Par& p, char* lds, int i0, int i1, int st, int lane) {
    for (int i = i0; i < i1; i += st) touch(i < 1024 ? mkd(p.wsp, 8 * MB) : i < 1196 ? mkd(p.win, 512 * 1376 * 2) : mkd(p.wout, 688 * 1024), lane * 128, (i < 1024 ? i : i < 1196 ? i - 1024 : i - 1196) * 8192, (unsigned)(size_t)lds + 159 * 1024);
}
DEVI void att(const Par& p, char* lds, int h, int w, int lane, rsrc_t rF, int& bud) {
    const int n = lane & 15, g = lane >> 4, fq = w & 1, eh = w >> 1;
    rsrc_t rP = mkr(p.ws + O_P2, 64 * MB);
    v4u W[4][2][4];
    wdma<1024>(p.Wo + h * 65536, 131072, w * 256, (unsigned)(size_t)lds + w * 32768, lane); drain();
    UNR for (int q = 0; q < 4; ++q) UNR for (int u = 0; u < 2; ++u) UNR for (int t = 0; t < 4; ++t) W[q][u][t] = trd(lds + w * 32768 + (8 * g + (n >> 2)) * 256 + (q ^ (n >> 2)) * 64 + (n & 3) * 16 + t * 8192 + 8 * u);
    bar();
    int kq = 0, km = 0; const int row = prow(n, fq);
    const unsigned fq3 = lane < 3 ? (F_QKV + lane * 32 + h) * 128 : OOB, fm4 = lane < 4 ? (F_REC + h * 4 + lane) * 128 : OOB, fb = lane < 4 ? fm4 : lane < 8 ? (F_QKV + min(lane - 4, 2) * 32 + h) * 128 : OOB, lb = (unsigned)(size_t)lds;
    const v4u dKV = mkd(p.ws + O_KV, 32 * MB), dQ = mkd(p.ws + O_QF, 16 * MB), dM = mkd(p.ws + O_KM, 16 * MB);
    v4u X[16], KT[2], VM;
    auto dmax = [&](int s, int i) __attribute__((always_inline)) { const unsigned b = lb + (s & 3) * 24576, u = (h * 64 + ((min(s, 63) + h) & 63)) * 4 + w;
        SB(); if (i < 4) dma(dKV, lane * 16, u * 4096, b + w * 4096, (i & 3) * 1024); else dma(dQ, lane * 16, u * 2048, b + 16384 + w * 2048, (i & 1) * 1024); SB(); };
    auto rdx = [&](int s, int i) { const char* b = lds + (s & 3) * 24576 + lane * 16;
        X[i] = *(const v4u*)(b + (i < 4 ? 16384 + i * 2048 + fq * 1024 : i < 12 ? ((i - 4) >> 1) * 4096 + (i & 1) * 1024 : (2 * eh + ((i - 12) >> 1)) * 4096 + 2048 + (i & 1) * 1024)); };
    auto loadm = [&](int s) __attribute__((always_inline)) { const unsigned u = (h * 64 + ((s + h) & 63)) * 8192, b = lb + 114688 + w * 3072;
        dma(dM, lane * 16 + (n >> 3) * 4096, u, b, 0); dma(dM, lane * 16 + (n >> 3) * 4096, u, b, 2048); dma(dM, lane * 16, u + eh * 4096, b, 1024); };
    auto both = [&](unsigned v) { v = min(v, (unsigned)__builtin_amdgcn_update_dpp(-1, v, 0xB1, 15, 15, false)); v = min(v, (unsigned)__builtin_amdgcn_update_dpp(-1, v, 0x4E, 15, 15, false));
        km = max(km, (int)__builtin_amdgcn_readlane(v, 0)); kq = max(kq, (int)__builtin_amdgcn_readlane(v, 4)); asm volatile("" ::: "memory"); };
    waitc(rF, fq3, 2, kq, bud);
    UNR for (int i = 0; i < 12; ++i) dmax(i / 6, i % 6);
    drain(); bar();
    UNR for (int i = 0; i < 16; ++i) rdx(0, i);
    int pre = 0; unsigned pv = 0; f32x4 sx[4], oa[2][2]; float mx, lx; constexpr float CE = 1.4426950408889634f;
    auto scl = [](f32x4& v) { UNR for (int r = 0; r < 4; r += 2) { unsigned t = pk(v[r], v[r + 1]); t = pk(lo(t) * 0.08838834764831845f, hi(t) * 0.08838834764831845f); v[r] = lo(t); v[r + 1] = hi(t); } };
    auto xp = [&](int k) __attribute__((always_inline)) {
        if (k == 0) { sx[0] = sx[1] = (f32x4){}; UNR for (int c = 0; c < 4; ++c) UNR for (int f = 0; f < 2; ++f) sx[f] = mf32(X[4 + 2 * c + f], X[c], sx[f]); }
        if (k == 1) { scl(sx[0]); scl(sx[1]); asm volatile("" : "+v"(sx[0]), "+v"(sx[1])); }
        if (k == 2) { UNR for (int f = 0; f < 2; ++f) UNR for (int r = 0; r < 4; ++r) sx[f][r] = 8 * g + 4 * f + r <= row ? sx[f][r] : -999292928.f;
            mx = gmax(fmaxf(fmaxf(fmaxf(sx[0][0], sx[0][1]), fmaxf(sx[0][2], sx[0][3])), fmaxf(fmaxf(sx[1][0], sx[1][1]), fmaxf(sx[1][2], sx[1][3])))) * CE; asm volatile("" : "+v"(sx[0]), "+v"(sx[1]), "+v"(mx)); }
        if (k == 3) { UNR for (int f = 0; f < 2; ++f) UNR for (int r = 0; r < 4; ++r) sx[f][r] = __builtin_amdgcn_exp2f(__builtin_fmaf(sx[f][r], CE, -mx)); asm volatile("" : "+v"(sx[0]), "+v"(sx[1])); }
        if (k == 4) { const v4u px = pk8(sx[0], sx[1]); UNR for (int i = 0; i < 4; ++i) oa[i >> 1][i & 1] = mf32(X[12 + i], px, (f32x4){});
            lx = ((sx[0][0] + sx[0][1]) + (sx[0][2] + sx[0][3])) + ((sx[1][0] + sx[1][1]) + (sx[1][2] + sx[1][3])); asm volatile("" : "+v"(lx)); }
        if (k == 5) asm volatile("" : "+v"(oa[0][0]), "+v"(oa[0][1]), "+v"(oa[1][0]), "+v"(oa[1][1])); };
    UNR for (int k = 0; k < 6; ++k) xp(k);
    TS0
    for (int s = 0; s < 64; ++s) {
        waitc(rF, fq3, min(s + 3, 64), kq, bud);
        if (!pre) { drain(); TA(6) st4c(rF, 0, (F_ATT + h * 4 + w) * 128, s); both(pv); Pol t = {}; if (s == 63 && (FW & 1)) waitf(p, fm4, km, t); waitc(rF, fm4, s + 1, km, bud); TA(1) loadm(s); drain(); KEEP(t) }
        else if (pre == 1) asm volatile("s_waitcnt vmcnt(%0)" :: "n"(8 - SK) : "memory");
        else asm volatile("s_waitcnt vmcnt(8)" ::: "memory");
        KT[0] = *(const v4u*)(lds + 114688 + w * 3072 + lane * 16); KT[1] = *(const v4u*)(lds + 116736 + w * 3072 + lane * 16); { const v2u a = *(const v2u*)(lds + 115712 + w * 3072 + lane * 8), b = *(const v2u*)(lds + 116224 + w * 3072 + lane * 8); VM = (v4u){a.x, a.y, b.x, b.y}; }
        const bool early = km > s + 1;
        if (early) { asm volatile("s_waitcnt lgkmcnt(0)" : "+v"(KT[0]), "+v"(KT[1]), "+v"(VM) :: "memory"); loadm(s + 1); }
        TA(0) dmax(s + 2, 0);
        sx[2] = sx[3] = (f32x4){};
        UNR for (int c = 0; c < 4; ++c) sx[2 + (c & 1)] = mf32(KT[c >> 1], X[(c >> 1) + 2 * (c & 1)], sx[2 + (c & 1)]);
        dmax(s + 2, 1);
        UNR for (int r = 0; r < 4; ++r) { const unsigned t = u_(g < 2 ? sx[2][r] : sx[3][r]); auto a = __builtin_amdgcn_permlane32_swap(t, t, false, false); sx[2][r] = f_(a[0]) + f_(a[1]); }
        scl(sx[2]);
        float m2 = fmaxf(fmaxf(sx[2][0], sx[2][1]), fmaxf(sx[2][2], sx[2][3]));
        dmax(s + 2, 2);
        { auto a = __builtin_amdgcn_permlane16_swap(u_(m2), u_(m2), false, false); m2 = fmaxf(fmaxf(f_(a[0]), f_(a[1])) * CE, mx); }
        float al = __builtin_amdgcn_exp2f(mx - m2);
        UNR for (int r = 0; r < 4; ++r) sx[2][r] = __builtin_amdgcn_exp2f(__builtin_fmaf(sx[2][r], CE, -m2));
        dmax(s + 2, 3);
        const float in = __builtin_amdgcn_rcpf(gsum(lx * al + 0.5f * ((sx[2][0] + sx[2][1]) + (sx[2][2] + sx[2][3])))); al *= in;
        dmax(s + 2, 4);
        const v2u pm = {pk(sx[2][0] * in, sx[2][1] * in), pk(sx[2][2] * in, sx[2][3] * in)}, z2 = {0, 0};
        char* ob = lds + 98304 + (s & 1) * 8192 + lane * 16;
        f32x4 om[4];
        UNR for (int c = 0; c < 4; ++c) om[c] = mf16((v2u){VM[c & 2], VM[c | 1]}, (g < 2) != (c & 1) ? pm : z2, (f32x4){});
        dmax(s + 2, 5);
        UNR for (int c = 0; c < 2; ++c) *(v4u*)(ob + (fq * 4 + 2 * eh + c) * 1024) = pk8(oa[c][0] * al + om[2 * c], oa[c][1] * al + om[2 * c + 1]);
        SB(); if (pre) { if (early) asm volatile("s_waitcnt vmcnt(9)" ::: "memory"); else asm volatile("s_waitcnt vmcnt(6)" ::: "memory"); st4c(rF, 0, (F_ATT + h * 4 + w) * 128, s); }
        pv = peek(rF, fb);
        TA(2) bar(); TA(3)
        v4u o[2][4];
        UNR for (int i = 0; i < 8; ++i) o[i >> 2][i & 3] = *(const v4u*)(ob + i * 1024);
        SB(); TA(4)
        f32x4 a0[8], a1[8];
        UNR for (int i = 0; i < 9; ++i) { const int q = i >> 1, f = i & 1;
            if (i < 4) UNR for (int k = 0; k < 4; ++k) rdx(s + 1, 4 * i + k);
            if (i < 8) { a0[i] = a1[i] = (f32x4){}; UNR for (int k = 0; k < 4; ++k) { a0[i] = mf32(W[q][0][k], o[f][k], a0[i]); a1[i] = mf32(W[q][1][k], o[f][k], a1[i]); } }
            if (i) st16c(rP, lane * 16, (((s * 2 + ((i - 1) & 1)) * 16 + 4 * w + ((i - 1) >> 1)) * 32 + h) * 1024, pk8(a0[i - 1], a1[i - 1]));
            if (i >= XK) xp(i - XK);
            if (i < 8) { __builtin_amdgcn_sched_group_barrier(8, 2, 0);
                UNR for (int k = 0; k < 14; ++k) { __builtin_amdgcn_sched_group_barrier(0x100, 1, 0); __builtin_amdgcn_sched_group_barrier(2, NV, 0); __builtin_amdgcn_sched_group_barrier(8, 1, 0); } }
            SB();
            if (i == SK) { both(pv); pre = early ? 2 : km > s + 1; if (pre == 1) loadm(s + 1); }
            if (i == 8 && !pre) pv = peek(rF, fb);
            SB(); TW(i) }
        TA(5)
    }
    drain(); st4c(rF, 0, (F_ATT + h * 4 + w) * 128, 64);
    TSE(0)
}
DEVI void redx(const Par& p, char* lds, int j, int w, int lane, rsrc_t rF, int& bud) {
    rsrc_t rP = mkr(p.ws + O_P2, 64 * MB), rX = mkr(p.ws + O_X1, 2 * MB);
    const int tid = w * 64 + lane, l2 = tid >> 2, col = 32 * (j & 15) + 8 * (l2 >> 4) + 2 * (tid & 3), xo = prow(l2 & 15, j >> 4) * 512 + col;
    const unsigned ga = *(const unsigned*)(p.ws + O_MOD + (1024 + col) * 2), fo = lane < 8 ? (F_ATT + (8 * w + lane) * 4 + ((j & 15) >> 2)) * 128 : OOB; int kn = 0; TS0
    for (int s = 0; s < 64; ++s) {
        const unsigned xv = *(const unsigned*)(p.x + s * 16384 + xo);
        Pol pt = {}; if (s == 63 && (FW & 2)) waitf(p, fo, kn, pt);
        waitc(rF, fo, s + 1, kn, bud);
        TS(0) v4u a[8]; f32x4 s0 = {}, s1 = {};
        UNR for (int i = 0; i < 8; ++i) a[i] = ld16c(rP, lane * 16 + (i & 3) * 1024, ((s * 32 + j) * 32 + 8 * w + (i & 4)) * 1024);
        SB();
        UNR for (int i = 0; i < 8; ++i) { s0 += (f32x4){lo(a[i].x), hi(a[i].x), lo(a[i].y), hi(a[i].y)}; s1 += (f32x4){lo(a[i].z), hi(a[i].z), lo(a[i].w), hi(a[i].w)}; }
        *(f32x4*)(lds + w * 2048 + lane * 32) = s0; *(f32x4*)(lds + w * 2048 + lane * 32 + 16) = s1;
        TS(1) bar(); KEEP(pt) TS(2)
        const f32x2* q = (const f32x2*)lds + tid; const f32x2 t = (q[0] + q[256]) + (q[512] + q[768]);
        unsigned y = pk(t[0], t[1]); y = pk(lo(ga) * lo(y), hi(ga) * hi(y));
        y = pk(lo(xv) + lo(y), hi(xv) + hi(y));
        st16c(rX, lane & 3 ? OOB : tid * 4, (s * 32 + j) * 1024, (v4u){y, (unsigned)__builtin_amdgcn_mov_dpp(y, 0x55, 15, 15, true), (unsigned)__builtin_amdgcn_mov_dpp(y, 0xAA, 15, 15, true), (unsigned)__builtin_amdgcn_mov_dpp(y, 0xFF, 15, 15, true)});
        const unsigned pv = peek(rF, fo);
        if (s == 58 && !(p.e1 & 1024)) touch(code_span(), lane * 128 + ((j >> 2) * 4 + w) * 8192, 0, (unsigned)(size_t)lds + 159 * 1024);
        TS(3) drain(); TS(4) bar(); TS(5)
        if (w == 0) st4c(rF, 0, (F_RED + j) * 128, s + 1);
        seen(pv, fo, kn);
    }
    TSE(0)
}
DEVI float gelu(float x) { x = rb(x); const float u = x * (-2.302208198144325f - 0.1029432397f * x * x); return x * __builtin_amdgcn_rcpf(1.f + __builtin_amdgcn_exp2f(u)); }
DEVI void win(const Par& p, char* lds, int L, int w, int lane, rsrc_t rF, int& bud) {
    const int n = lane & 15, g = lane >> 4, j = L >> 1, f = L & 1; const u16* mod = (const u16*)(p.ws + O_MOD); const bool Z2 = w >= 2;
    rsrc_t rX = mkr(p.ws + O_X1, 2 * MB), rZ = mkr(p.ws + (Z2 ? O_Z2 : O_Z1), 3 * MB), rS = mkr(p.ws + O_ST, MB);
    v4u W[8][4], id[2], x[4]; f32x4 cs[2] = {}, bi[2] = {}; const v4u ones = {0x3F803F80u, 0x3F803F80u, 0x3F803F80u, 0x3F803F80u};
    wdma<2752, 1376>(p.win, 512 * 2752, w * 128 * 2752 + j * 128, (unsigned)(size_t)lds + w * 32768, lane); drain(); bar();
    UNR for (int i = 0; i < 64; ++i) { const int P = i < 32 ? i & 15 : 4 * w + (i & 3), T = i < 32 ? 2 * w + (i >> 4) : (i >> 2) & 7;
        v4u t = trd(lds + (32 * P + 8 * g + (n >> 2)) * 256 + ((T >> 1) ^ (n >> 2)) * 64 + (n & 3) * 16 + 8 * (T & 1)); if (j == 10 && (T & 2) && n >= 8) t = (v4u){0, 0, 0, 0};
        const v4u sh = *(const v4u*)(mod + 1536 + 32 * P + 8 * g), c1 = plus1(*(const v4u*)(mod + 2048 + 32 * P + 8 * g));
        if (i < 32) bi[i >> 4] = mf32(Z2 ? sh : t, Z2 ? t : sh, bi[i >> 4]);
        UNR for (int q = 0; q < 4; ++q) t[q] = pk(lo(t[q]) * lo(c1[q]), hi(t[q]) * hi(c1[q]));
        if (i < 32) cs[i >> 4] = mf32(Z2 ? ones : t, Z2 ? t : ones, cs[i >> 4]); else W[(i >> 2) & 7][i & 3] = t;
        if ((i & 7) == 7) SB(); }
    bar();
    UNR for (int t = 0; t < 2; ++t) UNR for (int q = 0; q < 4; ++q) { const int k = n - 4 * g - 2 * (q & 1); id[t][q] = (q >> 1) != t ? 0u : k == 0 ? 0x3F80u : k == 1 ? 0x3F800000u : 0u; }
    const unsigned fo = lane < 4 ? (F_RED + f * 16 + 4 * w + lane) * 128 : OOB; int kn = 0; bool pre = false;
    float* sr = (float*)(lds + 33792); f32x2* red = (f32x2*)(lds + 32768); f32x2* my = (f32x2*)(lds + 34816 + w * 128); const f32x2* mr = my + (Z2 ? 4 * g : n); const int ms = Z2;
    auto ldx = [&](int s) { UNR for (int i = 0; i < 4; ++i) { x[i] = ld16c(rX, lane * 16 + i * 1024, (s * 32 + f * 16 + 4 * w) * 1024); SB(); } };
    TS0
    for (int s = 0; s < 64; ++s) {
        Pol pt = {};
        if (!pre) { if (s == 63 && (FW & 4)) waitf(p, fo, kn, pt); waitc(rF, fo, s + 1, kn, bud); TS(0) ldx(s); }
        SB(); unsigned pv;
        f32x4 a[8], u = {}, q = {};
        UNR for (int i = 0; i < 4; ++i) { u = mf32(ones, x[i], u); q = mf32(x[i], x[i], q);
            UNR for (int t = 0; t < 8; ++t) { const f32x4 c = i ? a[t] : (f32x4){}; a[t] = t >= 4 ? mf32(x[i], W[t][i], c) : mf32(W[t][i], x[i], c);
                if (i == 3 && t == 0) pv = peek(rF, fo);
                if (i == 3 && t >= 2) *(f32x4*)(lds + ((t - 2) * 4 + w) * 1024 + lane * 16) = a[t - 2];
                SB(); } }
        red[(n >> 2) == g ? w * 16 + n : 64 + lane] = (f32x2){u[0], n & 2 ? (n & 1 ? q[3] : q[2]) : (n & 1 ? q[1] : q[0])};
        UNR for (int t = 6; t < 8; ++t) *(f32x4*)(lds + (t * 4 + w) * 1024 + lane * 16) = a[t];
        if (pre && w == 2) st4c(rF, 0, (F_WIN + L) * 128, s);
        TS(1) bar(); KEEP(pt) TS(2)
        { const f32x2 t = (red[n] + red[16 + n]) + (red[32 + n] + red[48 + n]);
          UNR for (int i = 0; i < 8; ++i) a[i] = *(const f32x4*)(lds + (w * 8 + i) * 1024 + lane * 16);
          const float mean = t[0] * (1.f / 512), var = fmaxf(t[1] * (1.f / 512) - mean * mean, 0.f); my[n] = (f32x2){rb(mean), __builtin_amdgcn_rcpf(rb(__builtin_amdgcn_sqrtf(rb(rb(var) + 1e-5f))))}; }
        UNR for (int t = 0; t < 2; ++t) a[t] = (a[4 * t] + a[4 * t + 1]) + (a[4 * t + 2] + a[4 * t + 3]);
        UNR for (int r = 0; r < 4; ++r) { const f32x2 m = mr[ms * r]; UNR for (int t = 0; t < 2; ++t) a[t][r] = gelu((a[t][r] - m[0] * cs[t][r]) * m[1] + bi[t][r]); }
        const v4u z = pk8(a[0], a[1]);
        SB(); seen(pv, fo, kn); pre = kn > s + 1;
        st16c(rZ, lane * 16, ((s * 2 + f) * 22 + 2 * j + (w & 1)) * 1024, z); TS(3)
        if (pre && !Z2) ldx(s + 1);
        if (Z2) { const f32x4 d = mf32(z, id[0], (f32x4){}), e = mf32(z, id[1], (f32x4){}), t = d + e, v = d * d + e * e; const float uu = gsum((t[0] + t[1]) + (t[2] + t[3])), qq = gsum((v[0] + v[1]) + (v[2] + v[3]));
            if (g < 2) sr[(w & 1) * 32 + lane] = g ? qq : uu; }
        bar();
        if (w == 0) { const f32x4 t = ((const f32x4*)sr)[lane & 7] + ((const f32x4*)sr)[8 + (lane & 7)]; st16c(rS, lane < 8 ? (lane & 4) * 32 + (lane & 3) * 16 : OOB, s * 4096 + j * 256 + f * 64, (v4u){u_(t[0]), u_(t[1]), u_(t[2]), u_(t[3])}); }
        if (pre) { if (Z2) ldx(s + 1); } else pv = peek(rF, fo);
        TS(4) drain(); TS(5) bar(); TS(6)
        if (!pre) { if (w == 2) st4c(rF, 0, (F_WIN + L) * 128, s + 1); seen(pv, fo, kn); }
    }
    TSE(0)
}
DEVI void wtile(const Par& p, int u, int w, int lane, rsrc_t rF) {
    rsrc_t rO = mkr(p.ws + O_WS, 8 * MB);
    for (int i = u * 4 + w; i < 512; i += 256) { const int B = i >> 4, c = (i + B) & 15; v4u t[16];
        UNR for (int k = 0; k < 16; ++k) t[k] = *(const v4u*)(p.wsp + (B * 64 + ((k >> 1) & 1) * 32 + prow(lane & 15, k & 1)) * 2048 + (c * 4 + (k >> 2)) * 32 + 8 * (lane >> 4));
        SB();
        UNR for (int k = 0; k < 16; ++k) st16c(rO, lane * 16 + (k & 3) * 1024, (B * 64 + c * 4 + (k >> 2)) * 4096, t[k]); }
    drain(); bar();
    if (w == 0) st4c(rF, 0, (F_WS + u) * 128, 64);
}
DEVI void spat(const Par& p, char* lds, int H, int w, int lane, rsrc_t rF, int& bud) {
    const int n = lane & 15, g = lane >> 4, B = H & 31, P0 = 8 * (H >> 5) + 2 * w;
    rsrc_t rZ = mkr(p.ws + O_Z2, 3 * MB), rS = mkr(p.ws + O_ST, MB), rY = mkr(p.ws + O_Z1, 3 * MB);
    rsrc_t rW = mkr(p.ws + O_WS, 8 * MB); { int k0 = 0; waitc(rF, (F_WS + lane) * 128, 64, k0, bud); }
    const unsigned l5 = P0 < 22 ? lane * 16 : OOB;
    f32x4 a[4][4] = {}; float cr[4] = {0, 0, 0, 0}; int kn = 0; f32x2* sl = (f32x2*)(lds + w * 1024); volatile int* kl = (volatile int*)(lds + 4096);
    v4u wn[4], y[4][2], zn[4], sn[3]; bool pre = false; TS0
    auto ldz = [&](int s, unsigned m) { UNR for (int i = 0; i < 3; ++i) sn[i] = ld16c(rS, (i == 2 && g == 3 ? OOB : lane * 16 + i * 1024) | m, s * 4096);
        UNR for (int i = 0; i < 4; ++i) zn[i] = ld16c(rZ, (l5 + (i >> 1) * 1024) | m, (s * 44 + (i & 1) * 22 + P0) * 1024); };
    UNR for (int f = 0; f < 4; ++f) { wn[f] = ld16c(rW, lane * 16 + f * 1024, B * 262144); cr[f] = g ? 0.f : -lo(p.bsp[B * 64 + (f >> 1) * 32 + prow(n, f & 1)]); }
    for (int s = 0; s < 64; ++s) {
        Pol pt = {};
        if (!pre) { if (w == 0) { if (s == 63 && (FW & 8)) waitf(p, lane < 22 ? (F_WIN + lane) * 128 : OOB, kn, pt); waitc(rF, lane < 22 ? (F_WIN + lane) * 128 : OOB, s + 1, kn, bud); kl[s] = kn; } bar(); kn = __builtin_amdgcn_readfirstlane(kl[s]); TS(0) ldz(s, 0); }
        if (s == (B == 31 ? 63 : 61)) UNR for (int i = 0; i < 8; ++i) y[i >> 1][i & 1] = ld16c(rY, l5 + (i & 1) * 1024, ((B * 4 + (i >> 1)) * 22 + P0) * 1024);
        SB();
        v4u z[4], wc[4], wf[4]; unsigned t4[4];
        UNR for (int i = 0; i < 4; ++i) t4[i] = u_(f_(sn[0][i]) + f_(sn[1][i]) + f_(sn[2][i]));
        UNR for (int i = 0; i < 2; ++i) { auto sw = __builtin_amdgcn_permlane32_swap(t4[i], t4[i + 2], false, false); t4[i] = u_(f_(sw[0]) + f_(sw[1])); }
        auto sw = __builtin_amdgcn_permlane16_swap(t4[0], t4[1], false, false); const float st = f_(sw[0]) + f_(sw[1]);
        const float mean = st * (1.f / 688), var = fmaxf(f_(__builtin_amdgcn_mov_dpp(u_(st), 0x128, 15, 15, true)) * (1.f / 688) - mean * mean, 0.f);
        sl[n < 8 ? 8 * (n & 3) + (n & 4) + g : 32 + lane] = (f32x2){rb(mean), __builtin_amdgcn_rcpf(rb(__builtin_amdgcn_sqrtf(rb(rb(var) + 1e-5f))))};
        f32x4 m[4];
        UNR for (int q = 0; q < 4; ++q) { m[q] = *(const f32x4*)(sl + 8 * g + 2 * q); z[q] = (v4u){zn[q & 2][2 * (q & 1)], zn[q & 2][2 * (q & 1) + 1], zn[q | 1][2 * (q & 1)], zn[q | 1][2 * (q & 1) + 1]}; wc[q] = wn[q]; asm volatile("" : "+v"(z[q]), "+v"(wc[q])); }
        KEEP(pt) TS(1) pre = kn > s + 1;
        if (s < 63) { ldz(s + 1, pre ? 0 : OOB);
            UNR for (int f = 0; f < 4; ++f) wn[f] = ld16c(rW, lane * 16 + f * 1024, (B * 64 + s + 1) * 4096); }
        SB(); TS(4)
        const v4u mu = {pk(m[0][0], m[0][2]), pk(m[1][0], m[1][2]), pk(m[2][0], m[2][2]), pk(m[3][0], m[3][2])};
        UNR for (int f = 0; f < 4; ++f) {
            UNR for (int q = 0; q < 4; ++q) { const unsigned t = pk(lo(wc[f][q]) * m[q][1], hi(wc[f][q]) * m[q][3]); wf[f][q] = t; cr[f] = dot2(t, mu[q], cr[f]); }
            UNR for (int i = 0; i < 4; ++i) a[i][f] = mf32(z[i], wf[f], a[i][f]); }
        TS(2)
    }
    UNR for (int f = 0; f < 4; ++f) { const float c = gsum(cr[f]);
        UNR for (int i = 0; i < 2; ++i) { v4u o;
            UNR for (int q = 0; q < 4; ++q) { const f32x4 v = a[2 * i + (q >> 1)][f]; const unsigned zz = y[f][i][q];
                const unsigned t = pk(v[2 * (q & 1)] - c, v[2 * (q & 1) + 1] - c); o[q] = pk(lo(t) * lo(zz), hi(t) * hi(zz)); }
            if (P0 < 22) *(v4u*)(p.ws + O_UF + ((B * 4 + f) * 22 + P0 + i) * 1024 + lane * 16) = o; } }
    TS(3) TSE(1)
}
DEVI void reducer(const Par& p, int j, int lane, rsrc_t rF, int bud) {
    const int n = lane & 15, g = lane >> 4; unsigned side;
    if (j == 0) { rsrc_t rC = mkr(p.ws + O_RING + CALO, 16384); unsigned m[2] = {~0u, ~0u};
        for (int i = 0; i < 8; ++i) UNR for (int q = 0; q < 2; ++q) { const unsigned t0 = ct(); unsigned v = ld4c(rC, 0, q * 8192 + i * 1024); asm volatile("s_waitcnt vmcnt(0)" : "+v"(v) :: "memory"); m[q] = min(m[q], ct() - t0); }
        side = m[1] < m[0] ? 8193 : 1; st4c(rF, 0, 0, side); STAMP(3)
    } else for (;;) { asm volatile("" ::: "memory"); side = __builtin_amdgcn_readfirstlane(ld4c(rF, 0, 0)); if (side) break; __builtin_amdgcn_s_sleep(8); if (--bud < 0) return; }
    side = __builtin_amdgcn_readfirstlane(side); const char* rb_ = p.ws + O_RING + (side & 8192); rsrc_t rR = mkr(rb_, RING_BYTES); const v4u dR = mkd(rb_, RING_BYTES);
    v4u sel; { const int r = n & 7; const unsigned one = (r & 1) ? 0x3F800000u : 0x3F80u; UNR for (int q = 0; q < 4; ++q) sel[q] = (r >> 1) == q ? one : 0u; }
    const unsigned pubo = n < 8 ? ((j & 28) + 2 * (n >> 2) + ((j >> 1) & 1)) * 256 + ((g >> 1) * 8 + 4 * (j & 1) + (n & 3)) * 16 + (g & 1) * 8 : OOB; unsigned retr = 0;
    for (int s = 0; s < 63; ++s) {
        const unsigned so = ((s & 1) * 32 + j) * 16384; unsigned T;
        if (s && !p.e1) tcoarse(rt() + 50);
        for (;;) { asm volatile("" ::: "memory"); T = __builtin_amdgcn_readfirstlane(ld4c(rR, 0, SYNCO + s * 128)); if (T != SENT) break; __builtin_amdgcn_s_sleep(4); if (--bud < 0) return; }
        tcoarse(T - 25);
        for (;;) { unsigned long long bad;
            asm volatile("s_memrealtime s[20:21]\n s_waitcnt lgkmcnt(0)\n s_sub_i32 s20, %7, s20\n s_max_i32 s20, s20, 0\n s_min_i32 s20, s20, 60\n s_mul_i32 s20, s20, 55\n s_lshr_b32 s20, s20, 6\n"
                ".p2align 5\nW%=: s_sub_u32 s20, s20, 1\n s_cbranch_scc0 W%=\n"
                "buffer_load_dwordx4 v[0:3], %1, %4, %5 offen sc1\n buffer_load_dwordx4 v[4:7], %1, %4, %5 offen offset:1024 sc1\n buffer_load_dwordx4 v[8:11], %1, %4, %5 offen offset:2048 sc1\n buffer_load_dwordx4 v[12:15], %1, %4, %5 offen offset:3072 sc1\n"
                "s_add_u32 s20, %5, 4096\n buffer_load_dwordx4 v[16:19], %1, %4, s20 offen sc1\n buffer_load_dwordx4 v[20:23], %1, %4, s20 offen offset:1024 sc1\n buffer_load_dwordx4 v[24:27], %1, %4, s20 offen offset:2048 sc1\n buffer_load_dwordx4 v[28:31], %1, %4, s20 offen offset:3072 sc1\n"
                "s_waitcnt vmcnt(7)\n v_mfma_f32_16x16x32_bf16 v[32:35], v[0:3], %2, 0\n v_max_u32 v36, v0, v2\n"
                "s_waitcnt vmcnt(6)\n v_mfma_f32_16x16x32_bf16 v[32:35], v[4:7], %2, v[32:35]\n v_max3_u32 v36, v36, v4, v6\n"
                "s_waitcnt vmcnt(5)\n v_mfma_f32_16x16x32_bf16 v[32:35], v[8:11], %2, v[32:35]\n v_max3_u32 v36, v36, v8, v10\n"
                "s_waitcnt vmcnt(4)\n v_mfma_f32_16x16x32_bf16 v[32:35], v[12:15], %2, v[32:35]\n v_max3_u32 v36, v36, v12, v14\n"
                "s_waitcnt vmcnt(3)\n v_mfma_f32_16x16x32_bf16 v[32:35], v[16:19], %2, v[32:35]\n v_max3_u32 v36, v36, v16, v18\n"
                "s_waitcnt vmcnt(2)\n v_mfma_f32_16x16x32_bf16 v[32:35], v[20:23], %2, v[32:35]\n v_max3_u32 v36, v36, v20, v22\n"
                "s_waitcnt vmcnt(1)\n v_mfma_f32_16x16x32_bf16 v[32:35], v[24:27], %2, v[32:35]\n v_max3_u32 v36, v36, v24, v26\n"
                "s_waitcnt vmcnt(0)\n v_mfma_f32_16x16x32_bf16 v[32:35], v[28:31], %2, v[32:35]\n v_max3_u32 v36, v36, v28, v30\n"
                "v_cmp_eq_u32 vcc, -1, v36\n s_nop 1\n s_mov_b64 %0, vcc\n s_cbranch_vccnz S%=\n s_nop 1\n"
                "v_cvt_pk_bf16_f32 v32, v32, v33\n v_cvt_pk_bf16_f32 v33, v34, v35\n v_min_u32 v32, -2, v32\n buffer_store_dwordx2 v[32:33], %3, %4, %6 offen sc1\n S%=: s_nop 7"
                : "=s"(bad) : "v"(lane * 16), "v"(sel), "v"(pubo), "s"(dR), "s"(so), "s"(MBASE + ((s + 1) & 3) * 16384), "s"(T)
                : "v0","v1","v2","v3","v4","v5","v6","v7","v8","v9","v10","v11","v12","v13","v14","v15","v16","v17","v18","v19","v20","v21","v22","v23","v24","v25","v26","v27","v28","v29","v30","v31","v32","v33","v34","v35","v36","s20","s21","vcc","scc","memory");
            if (!bad) break;
            ++retr; if (--bud < 0) return;
        }
        tcoarse(rt() + (p.e1 & 255));
        st8c(rR, pubo, MBASE + ((s + 3) & 3) * 16384, (v2u){SENT, SENT});
        UNR for (int t = 0; t < 8; ++t) st16c(rR, lane * 16 + (t & 3) * 1024, so + (t & 4) * 1024, (v4u){SENT, SENT, SENT, SENT});
        drain();
    }
}
DEVI void recur(const Par& p, char* lds, int h, int w, int lane) {
    const int n = lane & 15, g = lane >> 4;
    const v4u rR = mkd(p.ws + O_RING, RING_BYTES), rKV = mkd(p.ws + O_KV, 32 * MB), rM = mkd(p.ws + O_KM, 16 * MB);
    const v4u rF = mkd(p.ws + O_FLG + (F_QKV + 32 + h) * 128, 8192), rG = mkd(p.ws + O_FLG, F_N * 128), rD = mkd(p.ws + O_DBG + 4096 + (h * 4 + w) * 4160, 65 * 64);
    const unsigned lb = (unsigned)(size_t)lds + lane * 16; const int k0 = (n & 7) - 4 * (g & 1);
    const v2u sel = {(k0 == 0 ? 0x3F80u : 0u) | (k0 == 1 ? 0x3F800000u : 0u), (k0 == 2 ? 0x3F80u : 0u) | (k0 == 3 ? 0x3F800000u : 0u)};
    const v4u ones = {0x3F803F80u, 0x3F803F80u, 0x3F803F80u, 0x3F803F80u};
    v4u oR, oV; int o1, o2, o3; const int r = n >> 2, c = n & 3;
    const v4u pa = {g * 8192u + (n ^ (4 * (g & 1))) * 16, (16 * (g >> 1) + 4 * (g & 1) + (r & 1) + 8 * (r >> 1)) * 256u + (w ^ r) * 64 + c * 16 - lane * 16,
        g * 1024u + (n ^ (4 * (g & 1))) * 16, lb - lane * 16 + w * 40960 + (4 * g + (r & 1)) * 256 + ((r & 1) + 2 * (g & 1)) * 64 + 32 * (r >> 1) + 8 * c};
    asm volatile(
"s_mov_b32 s62, 0x00ff00ff\n"
"s_mov_b32 s63, 0x00ff00ff\n"
"s_mov_b32 s64, 0xff00ff00\n"
"s_mov_b32 s65, 0xff00ff00\n"
"s_mov_b32 s85, 0x3e0293ee\n"
"s_mov_b32 s66, 0\n"
"s_mov_b32 s67, 400000\n"
"s_mov_b32 s84, 0\n"
"s_mov_b32 s93, 0\n"
"s_mov_b32 s69, 0\n"
"s_mov_b32 s70, 0\n"
"s_mov_b32 s76, s61\n"
"s_mov_b32 s81, 0\n"
"s_mov_b64 s[4:5], s[52:53]\n"
"s_mov_b32 s6, 4186368\n"
"s_mov_b32 s7, 0x20000\n"
"s_mov_b64 s[8:9], s[54:55]\n"
"s_mov_b32 s10, 4186368\n"
"s_mov_b32 s11, 0x20000\n"
"s_mov_b64 s[16:17], s[12:13]\n"
"s_mov_b32 s18, 4186368\n"
"s_mov_b32 s19, 0x20000\n"
"s_mov_b64 s[20:21], s[14:15]\n"
"s_mov_b32 s22, 131072\n"
"s_mov_b32 s23, 0x20000\n"
"v_readfirstlane_b32 s68, v225\n"
"s_lshl_b32 s72, s56, 20\n"
"v_xor_b32 v196, 0x80, v230\n"
"v_xor_b32 v197, 0x80, v232\n"
"v_add_u32 v192, v231, v225\n"
"v_xor_b32 v198, 0, v233\n"
"v_add_u32 v193, v231, v226\n"
"v_xor_b32 v199, 64, v233\n"
"v_add_u32 v194, v231, v227\n"
"v_xor_b32 v200, 128, v233\n"
"v_add_u32 v195, v231, v228\n"
"v_xor_b32 v201, 192, v233\n"
"s_add_u32 m0, s68, 8192\n"
"s_add_u32 s74, s72, 0\n"
"buffer_load_dwordx4 v230, s[4:7], s74 offen lds\n"
"s_add_u32 m0, s68, 9216\n"
"s_add_u32 s74, s72, 32768\n"
"buffer_load_dwordx4 v230, s[4:7], s74 offen lds\n"
"s_add_u32 m0, s68, 10240\n"
"s_add_u32 s74, s72, 65536\n"
"buffer_load_dwordx4 v196, s[4:7], s74 offen lds\n"
"s_add_u32 m0, s68, 11264\n"
"s_add_u32 s74, s72, 98304\n"
"buffer_load_dwordx4 v196, s[4:7], s74 offen lds\n"
"s_add_u32 m0, s68, 12288\n"
"s_add_u32 s74, s72, 131072\n"
"buffer_load_dwordx4 v230, s[4:7], s74 offen lds\n"
"s_add_u32 m0, s68, 13312\n"
"s_add_u32 s74, s72, 163840\n"
"buffer_load_dwordx4 v230, s[4:7], s74 offen lds\n"
"s_add_u32 m0, s68, 14336\n"
"s_add_u32 s74, s72, 196608\n"
"buffer_load_dwordx4 v196, s[4:7], s74 offen lds\n"
"s_add_u32 m0, s68, 15360\n"
"s_add_u32 s74, s72, 229376\n"
"buffer_load_dwordx4 v196, s[4:7], s74 offen lds\n"
"s_add_u32 m0, s68, 16384\n"
"s_add_u32 s74, s72, 262144\n"
"buffer_load_dwordx4 v230, s[4:7], s74 offen lds\n"
"s_add_u32 m0, s68, 17408\n"
"s_add_u32 s74, s72, 294912\n"
"buffer_load_dwordx4 v230, s[4:7], s74 offen lds\n"
"s_add_u32 m0, s68, 18432\n"
"s_add_u32 s74, s72, 327680\n"
"buffer_load_dwordx4 v196, s[4:7], s74 offen lds\n"
"s_add_u32 m0, s68, 19456\n"
"s_add_u32 s74, s72, 360448\n"
"buffer_load_dwordx4 v196, s[4:7], s74 offen lds\n"
"s_add_u32 m0, s68, 20480\n"
"s_add_u32 s74, s72, 393216\n"
"buffer_load_dwordx4 v230, s[4:7], s74 offen lds\n"
"s_add_u32 m0, s68, 21504\n"
"s_add_u32 s74, s72, 425984\n"
"buffer_load_dwordx4 v230, s[4:7], s74 offen lds\n"
"s_add_u32 m0, s68, 22528\n"
"s_add_u32 s74, s72, 458752\n"
"buffer_load_dwordx4 v196, s[4:7], s74 offen lds\n"
"s_add_u32 m0, s68, 23552\n"
"s_add_u32 s74, s72, 491520\n"
"buffer_load_dwordx4 v196, s[4:7], s74 offen lds\n"
"s_add_u32 m0, s68, 24576\n"
"s_add_u32 s74, s72, 524288\n"
"buffer_load_dwordx4 v230, s[4:7], s74 offen lds\n"
"s_add_u32 m0, s68, 25600\n"
"s_add_u32 s74, s72, 557056\n"
"buffer_load_dwordx4 v230, s[4:7], s74 offen lds\n"
"s_add_u32 m0, s68, 26624\n"
"s_add_u32 s74, s72, 589824\n"
"buffer_load_dwordx4 v196, s[4:7], s74 offen lds\n"
"s_add_u32 m0, s68, 27648\n"
"s_add_u32 s74, s72, 622592\n"
"buffer_load_dwordx4 v196, s[4:7], s74 offen lds\n"
"s_add_u32 m0, s68, 28672\n"
"s_add_u32 s74, s72, 655360\n"
"buffer_load_dwordx4 v230, s[4:7], s74 offen lds\n"
"s_add_u32 m0, s68, 29696\n"
"s_add_u32 s74, s72, 688128\n"
"buffer_load_dwordx4 v230, s[4:7], s74 offen lds\n"
"s_add_u32 m0, s68, 30720\n"
"s_add_u32 s74, s72, 720896\n"
"buffer_load_dwordx4 v196, s[4:7], s74 offen lds\n"
"s_add_u32 m0, s68, 31744\n"
"s_add_u32 s74, s72, 753664\n"
"buffer_load_dwordx4 v196, s[4:7], s74 offen lds\n"
"s_add_u32 m0, s68, 32768\n"
"s_add_u32 s74, s72, 786432\n"
"buffer_load_dwordx4 v230, s[4:7], s74 offen lds\n"
"s_add_u32 m0, s68, 33792\n"
"s_add_u32 s74, s72, 819200\n"
"buffer_load_dwordx4 v230, s[4:7], s74 offen lds\n"
"s_add_u32 m0, s68, 34816\n"
"s_add_u32 s74, s72, 851968\n"
"buffer_load_dwordx4 v196, s[4:7], s74 offen lds\n"
"s_add_u32 m0, s68, 35840\n"
"s_add_u32 s74, s72, 884736\n"
"buffer_load_dwordx4 v196, s[4:7], s74 offen lds\n"
"s_add_u32 m0, s68, 36864\n"
"s_add_u32 s74, s72, 917504\n"
"buffer_load_dwordx4 v230, s[4:7], s74 offen lds\n"
"s_add_u32 m0, s68, 37888\n"
"s_add_u32 s74, s72, 950272\n"
"buffer_load_dwordx4 v230, s[4:7], s74 offen lds\n"
"s_add_u32 m0, s68, 38912\n"
"s_add_u32 s74, s72, 983040\n"
"buffer_load_dwordx4 v196, s[4:7], s74 offen lds\n"
"s_add_u32 m0, s68, 39936\n"
"s_add_u32 s74, s72, 1015808\n"
"buffer_load_dwordx4 v196, s[4:7], s74 offen lds\n"
"s_waitcnt vmcnt(24) lgkmcnt(0)\n"
"s_barrier\n"
"s_add_u32 m0, s68, 0\n"
"s_add_u32 s74, s72, 0\n"
"buffer_load_dwordx4 v230, s[8:11], s74 offen lds\n"
"s_add_u32 m0, s68, 1024\n"
"s_add_u32 s74, s72, 32768\n"
"buffer_load_dwordx4 v230, s[8:11], s74 offen lds\n"
"s_add_u32 m0, s68, 2048\n"
"s_add_u32 s74, s72, 65536\n"
"buffer_load_dwordx4 v196, s[8:11], s74 offen lds\n"
"s_add_u32 m0, s68, 3072\n"
"s_add_u32 s74, s72, 98304\n"
"buffer_load_dwordx4 v196, s[8:11], s74 offen lds\n"
"s_add_u32 m0, s68, 4096\n"
"s_add_u32 s74, s72, 131072\n"
"buffer_load_dwordx4 v230, s[8:11], s74 offen lds\n"
"s_add_u32 m0, s68, 5120\n"
"s_add_u32 s74, s72, 163840\n"
"buffer_load_dwordx4 v230, s[8:11], s74 offen lds\n"
"s_add_u32 m0, s68, 6144\n"
"s_add_u32 s74, s72, 196608\n"
"buffer_load_dwordx4 v196, s[8:11], s74 offen lds\n"
"s_add_u32 m0, s68, 7168\n"
"s_add_u32 s74, s72, 229376\n"
"buffer_load_dwordx4 v196, s[8:11], s74 offen lds\n"
"ds_read_b64_tr_b16 v[0:1], v192 offset:8192\n"
"ds_read_b64_tr_b16 v[2:3], v192 offset:8704\n"
"ds_read_b64_tr_b16 v[8:9], v192 offset:8200\n"
"ds_read_b64_tr_b16 v[10:11], v192 offset:8712\n"
"ds_read_b64_tr_b16 a[0:1], v193 offset:8192\n"
"ds_read_b64_tr_b16 a[2:3], v193 offset:8704\n"
"ds_read_b64_tr_b16 a[8:9], v193 offset:8200\n"
"ds_read_b64_tr_b16 a[10:11], v193 offset:8712\n"
"ds_read_b64_tr_b16 a[32:33], v194 offset:8192\n"
"ds_read_b64_tr_b16 a[34:35], v194 offset:8704\n"
"ds_read_b64_tr_b16 a[40:41], v194 offset:8200\n"
"ds_read_b64_tr_b16 a[42:43], v194 offset:8712\n"
"ds_read_b64_tr_b16 a[64:65], v195 offset:8192\n"
"ds_read_b64_tr_b16 a[66:67], v195 offset:8704\n"
"ds_read_b64_tr_b16 a[72:73], v195 offset:8200\n"
"ds_read_b64_tr_b16 a[74:75], v195 offset:8712\n"
"s_waitcnt vmcnt(24) lgkmcnt(0)\n"
"s_barrier\n"
"s_add_u32 m0, s68, 8192\n"
"s_add_u32 s74, s72, 262144\n"
"buffer_load_dwordx4 v230, s[8:11], s74 offen lds\n"
"s_add_u32 m0, s68, 9216\n"
"s_add_u32 s74, s72, 294912\n"
"buffer_load_dwordx4 v230, s[8:11], s74 offen lds\n"
"s_add_u32 m0, s68, 10240\n"
"s_add_u32 s74, s72, 327680\n"
"buffer_load_dwordx4 v196, s[8:11], s74 offen lds\n"
"s_add_u32 m0, s68, 11264\n"
"s_add_u32 s74, s72, 360448\n"
"buffer_load_dwordx4 v196, s[8:11], s74 offen lds\n"
"s_add_u32 m0, s68, 12288\n"
"s_add_u32 s74, s72, 393216\n"
"buffer_load_dwordx4 v230, s[8:11], s74 offen lds\n"
"s_add_u32 m0, s68, 13312\n"
"s_add_u32 s74, s72, 425984\n"
"buffer_load_dwordx4 v230, s[8:11], s74 offen lds\n"
"s_add_u32 m0, s68, 14336\n"
"s_add_u32 s74, s72, 458752\n"
"buffer_load_dwordx4 v196, s[8:11], s74 offen lds\n"
"s_add_u32 m0, s68, 15360\n"
"s_add_u32 s74, s72, 491520\n"
"buffer_load_dwordx4 v196, s[8:11], s74 offen lds\n"
"ds_read_b64_tr_b16 v[4:5], v192 offset:16384\n"
"ds_read_b64_tr_b16 v[6:7], v192 offset:16896\n"
"ds_read_b64_tr_b16 v[12:13], v192 offset:16392\n"
"ds_read_b64_tr_b16 v[14:15], v192 offset:16904\n"
"ds_read_b64_tr_b16 a[4:5], v193 offset:16384\n"
"ds_read_b64_tr_b16 a[6:7], v193 offset:16896\n"
"ds_read_b64_tr_b16 a[12:13], v193 offset:16392\n"
"ds_read_b64_tr_b16 a[14:15], v193 offset:16904\n"
"ds_read_b64_tr_b16 a[36:37], v194 offset:16384\n"
"ds_read_b64_tr_b16 a[38:39], v194 offset:16896\n"
"ds_read_b64_tr_b16 a[44:45], v194 offset:16392\n"
"ds_read_b64_tr_b16 a[46:47], v194 offset:16904\n"
"ds_read_b64_tr_b16 a[68:69], v195 offset:16384\n"
"ds_read_b64_tr_b16 a[70:71], v195 offset:16896\n"
"ds_read_b64_tr_b16 a[76:77], v195 offset:16392\n"
"ds_read_b64_tr_b16 a[78:79], v195 offset:16904\n"
"s_waitcnt vmcnt(24) lgkmcnt(0)\n"
"s_barrier\n"
"s_add_u32 m0, s68, 16384\n"
"s_add_u32 s74, s72, 524288\n"
"buffer_load_dwordx4 v230, s[8:11], s74 offen lds\n"
"s_add_u32 m0, s68, 17408\n"
"s_add_u32 s74, s72, 557056\n"
"buffer_load_dwordx4 v230, s[8:11], s74 offen lds\n"
"s_add_u32 m0, s68, 18432\n"
"s_add_u32 s74, s72, 589824\n"
"buffer_load_dwordx4 v196, s[8:11], s74 offen lds\n"
"s_add_u32 m0, s68, 19456\n"
"s_add_u32 s74, s72, 622592\n"
"buffer_load_dwordx4 v196, s[8:11], s74 offen lds\n"
"s_add_u32 m0, s68, 20480\n"
"s_add_u32 s74, s72, 655360\n"
"buffer_load_dwordx4 v230, s[8:11], s74 offen lds\n"
"s_add_u32 m0, s68, 21504\n"
"s_add_u32 s74, s72, 688128\n"
"buffer_load_dwordx4 v230, s[8:11], s74 offen lds\n"
"s_add_u32 m0, s68, 22528\n"
"s_add_u32 s74, s72, 720896\n"
"buffer_load_dwordx4 v196, s[8:11], s74 offen lds\n"
"s_add_u32 m0, s68, 23552\n"
"s_add_u32 s74, s72, 753664\n"
"buffer_load_dwordx4 v196, s[8:11], s74 offen lds\n"
"ds_read_b64_tr_b16 v[16:17], v192 offset:24576\n"
"ds_read_b64_tr_b16 v[18:19], v192 offset:25088\n"
"ds_read_b64_tr_b16 v[24:25], v192 offset:24584\n"
"ds_read_b64_tr_b16 v[26:27], v192 offset:25096\n"
"ds_read_b64_tr_b16 a[16:17], v193 offset:24576\n"
"ds_read_b64_tr_b16 a[18:19], v193 offset:25088\n"
"ds_read_b64_tr_b16 a[24:25], v193 offset:24584\n"
"ds_read_b64_tr_b16 a[26:27], v193 offset:25096\n"
"ds_read_b64_tr_b16 a[48:49], v194 offset:24576\n"
"ds_read_b64_tr_b16 a[50:51], v194 offset:25088\n"
"ds_read_b64_tr_b16 a[56:57], v194 offset:24584\n"
"ds_read_b64_tr_b16 a[58:59], v194 offset:25096\n"
"ds_read_b64_tr_b16 a[80:81], v195 offset:24576\n"
"ds_read_b64_tr_b16 a[82:83], v195 offset:25088\n"
"ds_read_b64_tr_b16 a[88:89], v195 offset:24584\n"
"ds_read_b64_tr_b16 a[90:91], v195 offset:25096\n"
"s_waitcnt vmcnt(24) lgkmcnt(0)\n"
"s_barrier\n"
"s_add_u32 m0, s68, 24576\n"
"s_add_u32 s74, s72, 786432\n"
"buffer_load_dwordx4 v230, s[8:11], s74 offen lds\n"
"s_add_u32 m0, s68, 25600\n"
"s_add_u32 s74, s72, 819200\n"
"buffer_load_dwordx4 v230, s[8:11], s74 offen lds\n"
"s_add_u32 m0, s68, 26624\n"
"s_add_u32 s74, s72, 851968\n"
"buffer_load_dwordx4 v196, s[8:11], s74 offen lds\n"
"s_add_u32 m0, s68, 27648\n"
"s_add_u32 s74, s72, 884736\n"
"buffer_load_dwordx4 v196, s[8:11], s74 offen lds\n"
"s_add_u32 m0, s68, 28672\n"
"s_add_u32 s74, s72, 917504\n"
"buffer_load_dwordx4 v230, s[8:11], s74 offen lds\n"
"s_add_u32 m0, s68, 29696\n"
"s_add_u32 s74, s72, 950272\n"
"buffer_load_dwordx4 v230, s[8:11], s74 offen lds\n"
"s_add_u32 m0, s68, 30720\n"
"s_add_u32 s74, s72, 983040\n"
"buffer_load_dwordx4 v196, s[8:11], s74 offen lds\n"
"s_add_u32 m0, s68, 31744\n"
"s_add_u32 s74, s72, 1015808\n"
"buffer_load_dwordx4 v196, s[8:11], s74 offen lds\n"
"ds_read_b64_tr_b16 v[20:21], v192 offset:32768\n"
"ds_read_b64_tr_b16 v[22:23], v192 offset:33280\n"
"ds_read_b64_tr_b16 v[28:29], v192 offset:32776\n"
"ds_read_b64_tr_b16 v[30:31], v192 offset:33288\n"
"ds_read_b64_tr_b16 a[20:21], v193 offset:32768\n"
"ds_read_b64_tr_b16 a[22:23], v193 offset:33280\n"
"ds_read_b64_tr_b16 a[28:29], v193 offset:32776\n"
"ds_read_b64_tr_b16 a[30:31], v193 offset:33288\n"
"ds_read_b64_tr_b16 a[52:53], v194 offset:32768\n"
"ds_read_b64_tr_b16 a[54:55], v194 offset:33280\n"
"ds_read_b64_tr_b16 a[60:61], v194 offset:32776\n"
"ds_read_b64_tr_b16 a[62:63], v194 offset:33288\n"
"ds_read_b64_tr_b16 a[84:85], v195 offset:32768\n"
"ds_read_b64_tr_b16 a[86:87], v195 offset:33280\n"
"ds_read_b64_tr_b16 a[92:93], v195 offset:32776\n"
"ds_read_b64_tr_b16 a[94:95], v195 offset:33288\n"
"s_waitcnt vmcnt(24) lgkmcnt(0)\n"
"s_barrier\n"
"s_add_u32 m0, s68, 32768\n"
"s_add_u32 s74, s72, 0\n"
"buffer_load_dwordx4 v230, s[16:19], s74 offen lds\n"
"s_add_u32 m0, s68, 33792\n"
"s_add_u32 s74, s72, 32768\n"
"buffer_load_dwordx4 v230, s[16:19], s74 offen lds\n"
"s_add_u32 m0, s68, 34816\n"
"s_add_u32 s74, s72, 65536\n"
"buffer_load_dwordx4 v196, s[16:19], s74 offen lds\n"
"s_add_u32 m0, s68, 35840\n"
"s_add_u32 s74, s72, 98304\n"
"buffer_load_dwordx4 v196, s[16:19], s74 offen lds\n"
"s_add_u32 m0, s68, 36864\n"
"s_add_u32 s74, s72, 131072\n"
"buffer_load_dwordx4 v230, s[16:19], s74 offen lds\n"
"s_add_u32 m0, s68, 37888\n"
"s_add_u32 s74, s72, 163840\n"
"buffer_load_dwordx4 v230, s[16:19], s74 offen lds\n"
"s_add_u32 m0, s68, 38912\n"
"s_add_u32 s74, s72, 196608\n"
"buffer_load_dwordx4 v196, s[16:19], s74 offen lds\n"
"s_add_u32 m0, s68, 39936\n"
"s_add_u32 s74, s72, 229376\n"
"buffer_load_dwordx4 v196, s[16:19], s74 offen lds\n"
"ds_read_b64_tr_b16 a[96:97], v192 offset:0\n"
"ds_read_b64_tr_b16 a[98:99], v192 offset:512\n"
"ds_read_b64_tr_b16 a[104:105], v192 offset:8\n"
"ds_read_b64_tr_b16 a[106:107], v192 offset:520\n"
"ds_read_b64_tr_b16 v[32:33], v193 offset:0\n"
"ds_read_b64_tr_b16 v[34:35], v193 offset:512\n"
"ds_read_b64_tr_b16 v[40:41], v193 offset:8\n"
"ds_read_b64_tr_b16 v[42:43], v193 offset:520\n"
"ds_read_b64_tr_b16 a[128:129], v194 offset:0\n"
"ds_read_b64_tr_b16 a[130:131], v194 offset:512\n"
"ds_read_b64_tr_b16 a[136:137], v194 offset:8\n"
"ds_read_b64_tr_b16 a[138:139], v194 offset:520\n"
"ds_read_b64_tr_b16 a[160:161], v195 offset:0\n"
"ds_read_b64_tr_b16 a[162:163], v195 offset:512\n"
"ds_read_b64_tr_b16 a[168:169], v195 offset:8\n"
"ds_read_b64_tr_b16 a[170:171], v195 offset:520\n"
"s_waitcnt vmcnt(24) lgkmcnt(0)\n"
"s_barrier\n"
"s_add_u32 m0, s68, 0\n"
"s_add_u32 s74, s72, 262144\n"
"buffer_load_dwordx4 v230, s[16:19], s74 offen lds\n"
"s_add_u32 m0, s68, 1024\n"
"s_add_u32 s74, s72, 294912\n"
"buffer_load_dwordx4 v230, s[16:19], s74 offen lds\n"
"s_add_u32 m0, s68, 2048\n"
"s_add_u32 s74, s72, 327680\n"
"buffer_load_dwordx4 v196, s[16:19], s74 offen lds\n"
"s_add_u32 m0, s68, 3072\n"
"s_add_u32 s74, s72, 360448\n"
"buffer_load_dwordx4 v196, s[16:19], s74 offen lds\n"
"s_add_u32 m0, s68, 4096\n"
"s_add_u32 s74, s72, 393216\n"
"buffer_load_dwordx4 v230, s[16:19], s74 offen lds\n"
"s_add_u32 m0, s68, 5120\n"
"s_add_u32 s74, s72, 425984\n"
"buffer_load_dwordx4 v230, s[16:19], s74 offen lds\n"
"s_add_u32 m0, s68, 6144\n"
"s_add_u32 s74, s72, 458752\n"
"buffer_load_dwordx4 v196, s[16:19], s74 offen lds\n"
"s_add_u32 m0, s68, 7168\n"
"s_add_u32 s74, s72, 491520\n"
"buffer_load_dwordx4 v196, s[16:19], s74 offen lds\n"
"ds_read_b64_tr_b16 a[100:101], v192 offset:8192\n"
"ds_read_b64_tr_b16 a[102:103], v192 offset:8704\n"
"ds_read_b64_tr_b16 a[108:109], v192 offset:8200\n"
"ds_read_b64_tr_b16 a[110:111], v192 offset:8712\n"
"ds_read_b64_tr_b16 v[36:37], v193 offset:8192\n"
"ds_read_b64_tr_b16 v[38:39], v193 offset:8704\n"
"ds_read_b64_tr_b16 v[44:45], v193 offset:8200\n"
"ds_read_b64_tr_b16 v[46:47], v193 offset:8712\n"
"ds_read_b64_tr_b16 a[132:133], v194 offset:8192\n"
"ds_read_b64_tr_b16 a[134:135], v194 offset:8704\n"
"ds_read_b64_tr_b16 a[140:141], v194 offset:8200\n"
"ds_read_b64_tr_b16 a[142:143], v194 offset:8712\n"
"ds_read_b64_tr_b16 a[164:165], v195 offset:8192\n"
"ds_read_b64_tr_b16 a[166:167], v195 offset:8704\n"
"ds_read_b64_tr_b16 a[172:173], v195 offset:8200\n"
"ds_read_b64_tr_b16 a[174:175], v195 offset:8712\n"
"s_waitcnt vmcnt(24) lgkmcnt(0)\n"
"s_barrier\n"
"s_add_u32 m0, s68, 8192\n"
"s_add_u32 s74, s72, 524288\n"
"buffer_load_dwordx4 v230, s[16:19], s74 offen lds\n"
"s_add_u32 m0, s68, 9216\n"
"s_add_u32 s74, s72, 557056\n"
"buffer_load_dwordx4 v230, s[16:19], s74 offen lds\n"
"s_add_u32 m0, s68, 10240\n"
"s_add_u32 s74, s72, 589824\n"
"buffer_load_dwordx4 v196, s[16:19], s74 offen lds\n"
"s_add_u32 m0, s68, 11264\n"
"s_add_u32 s74, s72, 622592\n"
"buffer_load_dwordx4 v196, s[16:19], s74 offen lds\n"
"s_add_u32 m0, s68, 12288\n"
"s_add_u32 s74, s72, 655360\n"
"buffer_load_dwordx4 v230, s[16:19], s74 offen lds\n"
"s_add_u32 m0, s68, 13312\n"
"s_add_u32 s74, s72, 688128\n"
"buffer_load_dwordx4 v230, s[16:19], s74 offen lds\n"
"s_add_u32 m0, s68, 14336\n"
"s_add_u32 s74, s72, 720896\n"
"buffer_load_dwordx4 v196, s[16:19], s74 offen lds\n"
"s_add_u32 m0, s68, 15360\n"
"s_add_u32 s74, s72, 753664\n"
"buffer_load_dwordx4 v196, s[16:19], s74 offen lds\n"
"ds_read_b64_tr_b16 a[112:113], v192 offset:16384\n"
"ds_read_b64_tr_b16 a[114:115], v192 offset:16896\n"
"ds_read_b64_tr_b16 a[120:121], v192 offset:16392\n"
"ds_read_b64_tr_b16 a[122:123], v192 offset:16904\n"
"ds_read_b64_tr_b16 v[48:49], v193 offset:16384\n"
"ds_read_b64_tr_b16 v[50:51], v193 offset:16896\n"
"ds_read_b64_tr_b16 v[56:57], v193 offset:16392\n"
"ds_read_b64_tr_b16 v[58:59], v193 offset:16904\n"
"ds_read_b64_tr_b16 a[144:145], v194 offset:16384\n"
"ds_read_b64_tr_b16 a[146:147], v194 offset:16896\n"
"ds_read_b64_tr_b16 a[152:153], v194 offset:16392\n"
"ds_read_b64_tr_b16 a[154:155], v194 offset:16904\n"
"ds_read_b64_tr_b16 a[176:177], v195 offset:16384\n"
"ds_read_b64_tr_b16 a[178:179], v195 offset:16896\n"
"ds_read_b64_tr_b16 a[184:185], v195 offset:16392\n"
"ds_read_b64_tr_b16 a[186:187], v195 offset:16904\n"
"s_waitcnt vmcnt(24) lgkmcnt(0)\n"
"s_barrier\n"
"s_add_u32 m0, s68, 16384\n"
"s_add_u32 s74, s72, 786432\n"
"buffer_load_dwordx4 v230, s[16:19], s74 offen lds\n"
"s_add_u32 m0, s68, 17408\n"
"s_add_u32 s74, s72, 819200\n"
"buffer_load_dwordx4 v230, s[16:19], s74 offen lds\n"
"s_add_u32 m0, s68, 18432\n"
"s_add_u32 s74, s72, 851968\n"
"buffer_load_dwordx4 v196, s[16:19], s74 offen lds\n"
"s_add_u32 m0, s68, 19456\n"
"s_add_u32 s74, s72, 884736\n"
"buffer_load_dwordx4 v196, s[16:19], s74 offen lds\n"
"s_add_u32 m0, s68, 20480\n"
"s_add_u32 s74, s72, 917504\n"
"buffer_load_dwordx4 v230, s[16:19], s74 offen lds\n"
"s_add_u32 m0, s68, 21504\n"
"s_add_u32 s74, s72, 950272\n"
"buffer_load_dwordx4 v230, s[16:19], s74 offen lds\n"
"s_add_u32 m0, s68, 22528\n"
"s_add_u32 s74, s72, 983040\n"
"buffer_load_dwordx4 v196, s[16:19], s74 offen lds\n"
"s_add_u32 m0, s68, 23552\n"
"s_add_u32 s74, s72, 1015808\n"
"buffer_load_dwordx4 v196, s[16:19], s74 offen lds\n"
"ds_read_b64_tr_b16 a[116:117], v192 offset:24576\n"
"ds_read_b64_tr_b16 a[118:119], v192 offset:25088\n"
"ds_read_b64_tr_b16 a[124:125], v192 offset:24584\n"
"ds_read_b64_tr_b16 a[126:127], v192 offset:25096\n"
"ds_read_b64_tr_b16 v[52:53], v193 offset:24576\n"
"ds_read_b64_tr_b16 v[54:55], v193 offset:25088\n"
"ds_read_b64_tr_b16 v[60:61], v193 offset:24584\n"
"ds_read_b64_tr_b16 v[62:63], v193 offset:25096\n"
"ds_read_b64_tr_b16 a[148:149], v194 offset:24576\n"
"ds_read_b64_tr_b16 a[150:151], v194 offset:25088\n"
"ds_read_b64_tr_b16 a[156:157], v194 offset:24584\n"
"ds_read_b64_tr_b16 a[158:159], v194 offset:25096\n"
"ds_read_b64_tr_b16 a[180:181], v195 offset:24576\n"
"ds_read_b64_tr_b16 a[182:183], v195 offset:25088\n"
"ds_read_b64_tr_b16 a[188:189], v195 offset:24584\n"
"ds_read_b64_tr_b16 a[190:191], v195 offset:25096\n"
"s_waitcnt vmcnt(24) lgkmcnt(0)\n"
"s_barrier\n"
"s_add_u32 s73, s56, 0\n"
"s_and_b32 s73, s73, 3\n"
"s_lshl_b32 s73, s73, 7\n"
"s_add_u32 s73, s73, s56\n"
"s_lshl_b32 s73, s73, 8\n"
"s_add_u32 m0, s68, 24576\n"
"s_add_u32 s74, s73, 0\n"
"buffer_load_dwordx4 v232, s[20:23], s74 offen lds\n"
"s_add_u32 m0, s68, 25600\n"
"s_add_u32 s74, s73, 4096\n"
"buffer_load_dwordx4 v197, s[20:23], s74 offen lds\n"
"s_add_u32 m0, s68, 26624\n"
"s_add_u32 s74, s73, 8192\n"
"buffer_load_dwordx4 v232, s[20:23], s74 offen lds\n"
"s_add_u32 m0, s68, 27648\n"
"s_add_u32 s74, s73, 12288\n"
"buffer_load_dwordx4 v197, s[20:23], s74 offen lds\n"
"s_add_u32 m0, s68, 28672\n"
"s_add_u32 s74, s73, 16384\n"
"buffer_load_dwordx4 v232, s[20:23], s74 offen lds\n"
"s_add_u32 m0, s68, 29696\n"
"s_add_u32 s74, s73, 20480\n"
"buffer_load_dwordx4 v197, s[20:23], s74 offen lds\n"
"s_add_u32 m0, s68, 30720\n"
"s_add_u32 s74, s73, 24576\n"
"buffer_load_dwordx4 v232, s[20:23], s74 offen lds\n"
"s_add_u32 m0, s68, 31744\n"
"s_add_u32 s74, s73, 28672\n"
"buffer_load_dwordx4 v197, s[20:23], s74 offen lds\n"
"ds_read_b64_tr_b16 a[192:193], v192 offset:32768\n"
"ds_read_b64_tr_b16 a[194:195], v192 offset:33280\n"
"ds_read_b64_tr_b16 a[200:201], v192 offset:32776\n"
"ds_read_b64_tr_b16 a[202:203], v192 offset:33288\n"
"ds_read_b64_tr_b16 a[224:225], v193 offset:32768\n"
"ds_read_b64_tr_b16 a[226:227], v193 offset:33280\n"
"ds_read_b64_tr_b16 a[232:233], v193 offset:32776\n"
"ds_read_b64_tr_b16 a[234:235], v193 offset:33288\n"
"ds_read_b64_tr_b16 v[176:177], v194 offset:32768\n"
"ds_read_b64_tr_b16 v[178:179], v194 offset:33280\n"
"ds_read_b64_tr_b16 v[184:185], v194 offset:32776\n"
"ds_read_b64_tr_b16 v[186:187], v194 offset:33288\n"
"ds_read_b64_tr_b16 v[80:81], v195 offset:32768\n"
"ds_read_b64_tr_b16 v[82:83], v195 offset:33280\n"
"ds_read_b64_tr_b16 v[88:89], v195 offset:32776\n"
"ds_read_b64_tr_b16 v[90:91], v195 offset:33288\n"
"s_waitcnt vmcnt(24) lgkmcnt(0)\n"
"s_barrier\n"
"s_add_u32 s73, s56, 3\n"
"s_and_b32 s73, s73, 3\n"
"s_lshl_b32 s73, s73, 7\n"
"s_add_u32 s73, s73, s56\n"
"s_lshl_b32 s73, s73, 8\n"
"s_add_u32 m0, s68, 32768\n"
"s_add_u32 s74, s73, 0\n"
"buffer_load_dwordx4 v232, s[20:23], s74 offen lds\n"
"s_add_u32 m0, s68, 33792\n"
"s_add_u32 s74, s73, 4096\n"
"buffer_load_dwordx4 v197, s[20:23], s74 offen lds\n"
"s_add_u32 m0, s68, 34816\n"
"s_add_u32 s74, s73, 8192\n"
"buffer_load_dwordx4 v232, s[20:23], s74 offen lds\n"
"s_add_u32 m0, s68, 35840\n"
"s_add_u32 s74, s73, 12288\n"
"buffer_load_dwordx4 v197, s[20:23], s74 offen lds\n"
"s_add_u32 m0, s68, 36864\n"
"s_add_u32 s74, s73, 16384\n"
"buffer_load_dwordx4 v232, s[20:23], s74 offen lds\n"
"s_add_u32 m0, s68, 37888\n"
"s_add_u32 s74, s73, 20480\n"
"buffer_load_dwordx4 v197, s[20:23], s74 offen lds\n"
"s_add_u32 m0, s68, 38912\n"
"s_add_u32 s74, s73, 24576\n"
"buffer_load_dwordx4 v232, s[20:23], s74 offen lds\n"
"s_add_u32 m0, s68, 39936\n"
"s_add_u32 s74, s73, 28672\n"
"buffer_load_dwordx4 v197, s[20:23], s74 offen lds\n"
"ds_read_b64_tr_b16 a[196:197], v192 offset:0\n"
"ds_read_b64_tr_b16 a[198:199], v192 offset:512\n"
"ds_read_b64_tr_b16 a[204:205], v192 offset:8\n"
"ds_read_b64_tr_b16 a[206:207], v192 offset:520\n"
"ds_read_b64_tr_b16 a[228:229], v193 offset:0\n"
"ds_read_b64_tr_b16 a[230:231], v193 offset:512\n"
"ds_read_b64_tr_b16 a[236:237], v193 offset:8\n"
"ds_read_b64_tr_b16 a[238:239], v193 offset:520\n"
"ds_read_b64_tr_b16 v[180:181], v194 offset:0\n"
"ds_read_b64_tr_b16 v[182:183], v194 offset:512\n"
"ds_read_b64_tr_b16 v[188:189], v194 offset:8\n"
"ds_read_b64_tr_b16 v[190:191], v194 offset:520\n"
"ds_read_b64_tr_b16 v[84:85], v195 offset:0\n"
"ds_read_b64_tr_b16 v[86:87], v195 offset:512\n"
"ds_read_b64_tr_b16 v[92:93], v195 offset:8\n"
"ds_read_b64_tr_b16 v[94:95], v195 offset:520\n"
"s_waitcnt vmcnt(24) lgkmcnt(0)\n"
"s_barrier\n"
"s_add_u32 s73, s56, 1\n"
"s_and_b32 s73, s73, 3\n"
"s_lshl_b32 s73, s73, 7\n"
"s_add_u32 s73, s73, s56\n"
"s_lshl_b32 s73, s73, 8\n"
"s_add_u32 m0, s68, 0\n"
"s_add_u32 s74, s73, 0\n"
"buffer_load_dwordx4 v232, s[20:23], s74 offen lds\n"
"s_add_u32 m0, s68, 1024\n"
"s_add_u32 s74, s73, 4096\n"
"buffer_load_dwordx4 v197, s[20:23], s74 offen lds\n"
"s_add_u32 m0, s68, 2048\n"
"s_add_u32 s74, s73, 8192\n"
"buffer_load_dwordx4 v232, s[20:23], s74 offen lds\n"
"s_add_u32 m0, s68, 3072\n"
"s_add_u32 s74, s73, 12288\n"
"buffer_load_dwordx4 v197, s[20:23], s74 offen lds\n"
"s_add_u32 m0, s68, 4096\n"
"s_add_u32 s74, s73, 16384\n"
"buffer_load_dwordx4 v232, s[20:23], s74 offen lds\n"
"s_add_u32 m0, s68, 5120\n"
"s_add_u32 s74, s73, 20480\n"
"buffer_load_dwordx4 v197, s[20:23], s74 offen lds\n"
"s_add_u32 m0, s68, 6144\n"
"s_add_u32 s74, s73, 24576\n"
"buffer_load_dwordx4 v232, s[20:23], s74 offen lds\n"
"s_add_u32 m0, s68, 7168\n"
"s_add_u32 s74, s73, 28672\n"
"buffer_load_dwordx4 v197, s[20:23], s74 offen lds\n"
"ds_read_b64_tr_b16 a[208:209], v192 offset:8192\n"
"ds_read_b64_tr_b16 a[210:211], v192 offset:8704\n"
"ds_read_b64_tr_b16 a[216:217], v192 offset:8200\n"
"ds_read_b64_tr_b16 a[218:219], v192 offset:8712\n"
"ds_read_b64_tr_b16 a[240:241], v193 offset:8192\n"
"ds_read_b64_tr_b16 a[242:243], v193 offset:8704\n"
"ds_read_b64_tr_b16 a[248:249], v193 offset:8200\n"
"ds_read_b64_tr_b16 a[250:251], v193 offset:8712\n"
"ds_read_b64_tr_b16 v[64:65], v194 offset:8192\n"
"ds_read_b64_tr_b16 v[66:67], v194 offset:8704\n"
"ds_read_b64_tr_b16 v[72:73], v194 offset:8200\n"
"ds_read_b64_tr_b16 v[74:75], v194 offset:8712\n"
"ds_read_b64_tr_b16 v[96:97], v195 offset:8192\n"
"ds_read_b64_tr_b16 v[98:99], v195 offset:8704\n"
"ds_read_b64_tr_b16 v[104:105], v195 offset:8200\n"
"ds_read_b64_tr_b16 v[106:107], v195 offset:8712\n"
"s_waitcnt vmcnt(24) lgkmcnt(0)\n"
"s_barrier\n"
"s_add_u32 s73, s56, 2\n"
"s_and_b32 s73, s73, 3\n"
"s_lshl_b32 s73, s73, 7\n"
"s_add_u32 s73, s73, s56\n"
"s_lshl_b32 s73, s73, 8\n"
"s_add_u32 m0, s68, 8192\n"
"s_add_u32 s74, s73, 0\n"
"buffer_load_dwordx4 v232, s[20:23], s74 offen lds\n"
"s_add_u32 m0, s68, 9216\n"
"s_add_u32 s74, s73, 4096\n"
"buffer_load_dwordx4 v197, s[20:23], s74 offen lds\n"
"s_add_u32 m0, s68, 10240\n"
"s_add_u32 s74, s73, 8192\n"
"buffer_load_dwordx4 v232, s[20:23], s74 offen lds\n"
"s_add_u32 m0, s68, 11264\n"
"s_add_u32 s74, s73, 12288\n"
"buffer_load_dwordx4 v197, s[20:23], s74 offen lds\n"
"s_add_u32 m0, s68, 12288\n"
"s_add_u32 s74, s73, 16384\n"
"buffer_load_dwordx4 v232, s[20:23], s74 offen lds\n"
"s_add_u32 m0, s68, 13312\n"
"s_add_u32 s74, s73, 20480\n"
"buffer_load_dwordx4 v197, s[20:23], s74 offen lds\n"
"s_add_u32 m0, s68, 14336\n"
"s_add_u32 s74, s73, 24576\n"
"buffer_load_dwordx4 v232, s[20:23], s74 offen lds\n"
"s_add_u32 m0, s68, 15360\n"
"s_add_u32 s74, s73, 28672\n"
"buffer_load_dwordx4 v197, s[20:23], s74 offen lds\n"
"ds_read_b64_tr_b16 a[212:213], v192 offset:16384\n"
"ds_read_b64_tr_b16 a[214:215], v192 offset:16896\n"
"ds_read_b64_tr_b16 a[220:221], v192 offset:16392\n"
"ds_read_b64_tr_b16 a[222:223], v192 offset:16904\n"
"ds_read_b64_tr_b16 a[244:245], v193 offset:16384\n"
"ds_read_b64_tr_b16 a[246:247], v193 offset:16896\n"
"ds_read_b64_tr_b16 a[252:253], v193 offset:16392\n"
"ds_read_b64_tr_b16 a[254:255], v193 offset:16904\n"
"ds_read_b64_tr_b16 v[68:69], v194 offset:16384\n"
"ds_read_b64_tr_b16 v[70:71], v194 offset:16896\n"
"ds_read_b64_tr_b16 v[76:77], v194 offset:16392\n"
"ds_read_b64_tr_b16 v[78:79], v194 offset:16904\n"
"ds_read_b64_tr_b16 v[100:101], v195 offset:16384\n"
"ds_read_b64_tr_b16 v[102:103], v195 offset:16896\n"
"ds_read_b64_tr_b16 v[108:109], v195 offset:16392\n"
"ds_read_b64_tr_b16 v[110:111], v195 offset:16904\n"
"s_waitcnt vmcnt(24) lgkmcnt(0)\n"
"s_barrier\n"
"ds_read_b64_tr_b16 v[112:113], v198 offset:24576\n"
"ds_read_b64_tr_b16 v[114:115], v198 offset:25088\n"
"ds_read_b64_tr_b16 v[116:117], v198 offset:28672\n"
"ds_read_b64_tr_b16 v[118:119], v198 offset:29184\n"
"ds_read_b64_tr_b16 v[128:129], v199 offset:24576\n"
"ds_read_b64_tr_b16 v[130:131], v199 offset:25088\n"
"ds_read_b64_tr_b16 v[132:133], v199 offset:28672\n"
"ds_read_b64_tr_b16 v[134:135], v199 offset:29184\n"
"ds_read_b64_tr_b16 v[144:145], v200 offset:24576\n"
"ds_read_b64_tr_b16 v[146:147], v200 offset:25088\n"
"ds_read_b64_tr_b16 v[148:149], v200 offset:28672\n"
"ds_read_b64_tr_b16 v[150:151], v200 offset:29184\n"
"ds_read_b64_tr_b16 v[160:161], v201 offset:24576\n"
"ds_read_b64_tr_b16 v[162:163], v201 offset:25088\n"
"ds_read_b64_tr_b16 v[164:165], v201 offset:28672\n"
"ds_read_b64_tr_b16 v[166:167], v201 offset:29184\n"
"s_waitcnt vmcnt(16) lgkmcnt(0)\n"
"ds_read_b64_tr_b16 v[120:121], v198 offset:32768\n"
"ds_read_b64_tr_b16 v[122:123], v198 offset:33280\n"
"ds_read_b64_tr_b16 v[124:125], v198 offset:36864\n"
"ds_read_b64_tr_b16 v[126:127], v198 offset:37376\n"
"ds_read_b64_tr_b16 v[136:137], v199 offset:32768\n"
"ds_read_b64_tr_b16 v[138:139], v199 offset:33280\n"
"ds_read_b64_tr_b16 v[140:141], v199 offset:36864\n"
"ds_read_b64_tr_b16 v[142:143], v199 offset:37376\n"
"ds_read_b64_tr_b16 v[152:153], v200 offset:32768\n"
"ds_read_b64_tr_b16 v[154:155], v200 offset:33280\n"
"ds_read_b64_tr_b16 v[156:157], v200 offset:36864\n"
"ds_read_b64_tr_b16 v[158:159], v200 offset:37376\n"
"ds_read_b64_tr_b16 v[168:169], v201 offset:32768\n"
"ds_read_b64_tr_b16 v[170:171], v201 offset:33280\n"
"ds_read_b64_tr_b16 v[172:173], v201 offset:36864\n"
"ds_read_b64_tr_b16 v[174:175], v201 offset:37376\n"
"s_waitcnt vmcnt(8) lgkmcnt(0)\n"
"ds_write_b128 v225, v[0:3] offset:16384\n"
"ds_write_b128 v225, v[4:7] offset:17408\n"
"ds_write_b128 v225, v[8:11] offset:18432\n"
"ds_write_b128 v225, v[12:15] offset:19456\n"
"ds_write_b128 v225, v[16:19] offset:20480\n"
"ds_write_b128 v225, v[20:23] offset:21504\n"
"ds_write_b128 v225, v[24:27] offset:22528\n"
"ds_write_b128 v225, v[28:31] offset:23552\n"
"ds_write_b128 v225, v[32:35] offset:24576\n"
"ds_write_b128 v225, v[36:39] offset:25600\n"
"ds_write_b128 v225, v[40:43] offset:26624\n"
"ds_write_b128 v225, v[44:47] offset:27648\n"
"ds_write_b128 v225, v[48:51] offset:28672\n"
"ds_write_b128 v225, v[52:55] offset:29696\n"
"ds_write_b128 v225, v[56:59] offset:30720\n"
"ds_write_b128 v225, v[60:63] offset:31744\n"
"ds_write_b128 v225, v[176:179] offset:32768\n"
"ds_write_b128 v225, v[180:183] offset:33792\n"
"ds_write_b128 v225, v[184:187] offset:34816\n"
"ds_write_b128 v225, v[188:191] offset:35840\n"
"ds_read_b64_tr_b16 v[0:1], v198 offset:0\n"
"ds_read_b64_tr_b16 v[2:3], v198 offset:512\n"
"ds_read_b64_tr_b16 v[4:5], v198 offset:4096\n"
"ds_read_b64_tr_b16 v[6:7], v198 offset:4608\n"
"ds_read_b64_tr_b16 v[8:9], v199 offset:0\n"
"ds_read_b64_tr_b16 v[10:11], v199 offset:512\n"
"ds_read_b64_tr_b16 v[12:13], v199 offset:4096\n"
"ds_read_b64_tr_b16 v[14:15], v199 offset:4608\n"
"ds_read_b64_tr_b16 v[16:17], v200 offset:0\n"
"ds_read_b64_tr_b16 v[18:19], v200 offset:512\n"
"ds_read_b64_tr_b16 v[20:21], v200 offset:4096\n"
"ds_read_b64_tr_b16 v[22:23], v200 offset:4608\n"
"ds_read_b64_tr_b16 v[24:25], v201 offset:0\n"
"ds_read_b64_tr_b16 v[26:27], v201 offset:512\n"
"ds_read_b64_tr_b16 v[28:29], v201 offset:4096\n"
"ds_read_b64_tr_b16 v[30:31], v201 offset:4608\n"
"s_waitcnt vmcnt(0) lgkmcnt(0)\n"
"ds_read_b64_tr_b16 v[32:33], v198 offset:8192\n"
"ds_read_b64_tr_b16 v[34:35], v198 offset:8704\n"
"ds_read_b64_tr_b16 v[36:37], v198 offset:12288\n"
"ds_read_b64_tr_b16 v[38:39], v198 offset:12800\n"
"ds_read_b64_tr_b16 v[40:41], v199 offset:8192\n"
"ds_read_b64_tr_b16 v[42:43], v199 offset:8704\n"
"ds_read_b64_tr_b16 v[44:45], v199 offset:12288\n"
"ds_read_b64_tr_b16 v[46:47], v199 offset:12800\n"
"ds_read_b64_tr_b16 v[48:49], v200 offset:8192\n"
"ds_read_b64_tr_b16 v[50:51], v200 offset:8704\n"
"ds_read_b64_tr_b16 v[52:53], v200 offset:12288\n"
"ds_read_b64_tr_b16 v[54:55], v200 offset:12800\n"
"ds_read_b64_tr_b16 v[56:57], v201 offset:8192\n"
"ds_read_b64_tr_b16 v[58:59], v201 offset:8704\n"
"ds_read_b64_tr_b16 v[60:61], v201 offset:12288\n"
"ds_read_b64_tr_b16 v[62:63], v201 offset:12800\n"
"s_waitcnt lgkmcnt(0)\n"
"ds_write_b128 v225, v[0:3] offset:0\n"
"ds_write_b128 v225, v[4:7] offset:1024\n"
"ds_write_b128 v225, v[8:11] offset:2048\n"
"ds_write_b128 v225, v[12:15] offset:3072\n"
"ds_write_b128 v225, v[16:19] offset:4096\n"
"ds_write_b128 v225, v[20:23] offset:5120\n"
"ds_write_b128 v225, v[24:27] offset:6144\n"
"ds_write_b128 v225, v[28:31] offset:7168\n"
"ds_write_b128 v225, v[32:35] offset:8192\n"
"ds_write_b128 v225, v[36:39] offset:9216\n"
"ds_write_b128 v225, v[40:43] offset:10240\n"
"ds_write_b128 v225, v[44:47] offset:11264\n"
"ds_write_b128 v225, v[48:51] offset:12288\n"
"ds_write_b128 v225, v[52:55] offset:13312\n"
"ds_write_b128 v225, v[56:59] offset:14336\n"
"ds_write_b128 v225, v[60:63] offset:15360\n"
"s_waitcnt lgkmcnt(0)\n"
"s_lshl_b32 s90, s56, 17\n"
"s_lshl_b32 s91, s59, 2\n"
"s_add_u32 s91, s91, s56\n"
"s_lshl_b32 s71, s91, 7\n"
"s_add_u32 s71, s71, 16384\n"
"s_lshl_b32 s91, s59, 8\n"
"s_add_u32 s91, s91, s56\n"
"s_lshl_b32 s92, s91, 11\n"
"s_lshl_b32 s91, s91, 12\n"
"SELW%=:\n"
"s_sub_u32 s67, s67, 1\n"
"s_cbranch_scc1 DONE%=\n"
"buffer_load_dword v229, off, s[32:35], 0 offset:0 sc1\n"
"s_waitcnt vmcnt(0)\n"
"v_readfirstlane_b32 s80, v229\n"
"s_nop 3\n"
"s_cmp_eq_u32 s80, 0\n"
"s_cbranch_scc1 SELW%=\n"
"s_and_b32 s80, s80, 8192\n"
"s_add_u32 s40, s40, s80\n"
"s_addc_u32 s41, s41, 0\n"
"s_mov_b64 s[24:25], s[40:41]\n"
"s_mov_b32 s27, s43\n"
"s_cmp_eq_u32 s91, 0\n"
"s_cselect_b32 s26, s42, 0\n"
"v_lshlrev_b32 v232, 8, v217\n"
"s_memtime s[98:99]\n"
"s_memrealtime s[94:95]\n"
"s_waitcnt lgkmcnt(0)\n"
"s_mov_b32 s86, s94\n"
"s_mov_b32 s82, s94\n"
"s_branch KVCHK%=\n"
".p2align 6\n"
"LOOP%=:\n"
"buffer_load_dwordx4 v[0:3], v217, s[40:43], s68 offen offset:0 sc1\n"
"buffer_load_dwordx4 v[4:7], v217, s[40:43], s68 offen offset:1024 sc1\n"
"s_add_u32 s77, s86, s61\n"
"s_add_u32 s77, s77, s57\n"
"v_mov_b32 v234, s77\n"
"s_add_u32 s77, s77, s58\n"
"v_mov_b32 v235, s77\n"
"v_mov_b32 v230, s66\n"
"s_lshl_b32 s80, s66, 7\n"
"s_add_u32 s87, s80, 1114112\n"
"s_cmp_eq_u32 s66, 63\n"
"s_waitcnt vmcnt(0)\n"
"v_max3_u32 v229, v0, v2, v4\n"
"v_max_u32 v229, v229, v6\n"
"v_cmp_eq_u32 vcc, -1, v229\n"
"s_cbranch_vccnz RETRYG%=\n"
"s_cbranch_scc1 LASTB%=\n"
"v_smfmac_f32_16x16x64_bf16 v[180:183], v[0:3], v[40:47], v216\n"
"ds_write_b128 v225, v[4:7] offset:37888\n"
"v_smfmac_f32_16x16x64_bf16 v[176:179], v[0:3], v[32:39], v216\n"
"ds_write_b128 v225, v[0:3] offset:36864\n"
"v_smfmac_f32_16x16x64_bf16 v[192:195], v[0:3], a[192:199], v216\n"
"buffer_store_dwordx2 v[234:235], off, s[24:27], s87 sc1\n"
"v_smfmac_f32_16x16x64_bf16 v[184:187], v[0:3], a[96:103], v216\n"
"ds_read_b128 v[36:39], v225 offset:25600\n"
"v_smfmac_f32_16x16x64_bf16 v[176:179], v[4:7], v[48:55], v216\n"
"v_smfmac_f32_16x16x64_bf16 v[188:191], v[0:3], a[104:111], v216\n"
"s_waitcnt lgkmcnt(1)\n"
"s_barrier\n"
"v_smfmac_f32_16x16x64_bf16 v[196:199], v[0:3], a[200:207], v216\n"
"ds_read_b128 v[8:11], v226 offset:36864\n"
"v_smfmac_f32_16x16x64_bf16 v[192:195], v[4:7], a[208:215], v216\n"
"ds_read_b128 v[12:15], v226 offset:37888\n"
"v_smfmac_f32_16x16x64_bf16 v[184:187], v[4:7], a[112:119], v216\n"
"ds_read_b128 v[20:23], v227 offset:37888\n"
"v_smfmac_f32_16x16x64_bf16 v[188:191], v[4:7], a[120:127], v216\n"
"ds_read_b128 v[16:19], v227 offset:36864\n"
"v_smfmac_f32_16x16x64_bf16 v[180:183], v[4:7], v[56:63], v216\n"
"ds_read_b128 v[56:59], v225 offset:30720\n"
"s_waitcnt lgkmcnt(4)\n"
"v_smfmac_f32_16x16x64_bf16 v[180:183], v[8:11], a[8:15], v216\n"
"ds_read_b128 v[32:35], v225 offset:24576\n"
"s_waitcnt lgkmcnt(4)\n"
"v_smfmac_f32_16x16x64_bf16 v[180:183], v[12:15], a[24:31], v216\n"
"buffer_load_dword v233, v232, s[36:39], 0 offen sc1\n"
"v_smfmac_f32_16x16x64_bf16 v[176:179], v[8:11], a[0:7], v216\n"
"ds_read_b128 v[24:27], v228 offset:36864\n"
"v_smfmac_f32_16x16x64_bf16 v[176:179], v[12:15], a[16:23], v216\n"
"ds_read_b128 v[28:31], v228 offset:37888\n"
"s_waitcnt lgkmcnt(4)\n"
"v_smfmac_f32_16x16x64_bf16 v[180:183], v[16:19], a[40:47], v216\n"
"ds_read_b128 v[60:63], v225 offset:31744\n"
"v_smfmac_f32_16x16x64_bf16 v[176:179], v[16:19], a[32:39], v216\n"
"ds_read_b128 v[40:43], v225 offset:26624\n"
"v_smfmac_f32_16x16x64_bf16 v[180:183], v[20:23], a[56:63], v216\n"
"ds_read_b128 v[44:47], v225 offset:27648\n"
"s_waitcnt lgkmcnt(4)\n"
"v_smfmac_f32_16x16x64_bf16 v[180:183], v[24:27], a[72:79], v216\n"
"ds_read_b128 v[48:51], v225 offset:28672\n"
"s_waitcnt lgkmcnt(4)\n"
"v_smfmac_f32_16x16x64_bf16 v[180:183], v[28:31], a[88:95], v216\n"
"buffer_load_dword v252, v253, s[44:47], s88 offen sc1\n"
"v_smfmac_f32_16x16x64_bf16 v[176:179], v[20:23], a[48:55], v216\n"
"ds_read_b128 v[52:55], v225 offset:29696\n"
"v_smfmac_f32_16x16x64_bf16 v[176:179], v[24:27], a[64:71], v216\n"
"v_smfmac_f32_16x16x64_bf16 v[176:179], v[28:31], a[80:87], v216\n"
"s_waitcnt lgkmcnt(2)\n"
"v_smfmac_f32_16x16x64_bf16 v[188:191], v[8:11], v[40:47], v216\n"
"v_cvt_pk_bf16_f32 v238, v180, v181\n"
"v_smfmac_f32_16x16x64_bf16 v[184:187], v[8:11], v[32:39], v216\n"
"ds_read_b128 v[36:39], v225 offset:33792\n"
"v_cvt_pk_bf16_f32 v239, v182, v183\n"
"v_smfmac_f32_16x16x64_bf16 v[188:191], v[12:15], v[56:63], v216\n"
"ds_read_b128 v[0:3], v225 offset:8192\n"
"s_waitcnt lgkmcnt(2)\n"
"v_smfmac_f32_16x16x64_bf16 v[184:187], v[12:15], v[48:55], v216\n"
"ds_read_b128 v[48:51], v225 offset:0\n"
"v_cvt_pk_bf16_f32 v237, v178, v179\n"
"v_mfma_f32_16x16x16_bf16 v[180:183], v[238:239], v[218:219], 0\n"
"v_cvt_pk_bf16_f32 v236, v176, v177\n"
"v_smfmac_f32_16x16x64_bf16 v[188:191], v[16:19], a[136:143], v216\n"
"ds_read_b128 v[56:59], v225 offset:2048\n"
"v_mfma_f32_16x16x16_bf16 v[176:179], v[236:237], v[218:219], 0\n"
"s_nop 1\n"
"v_cvt_pk_bf16_f32 v242, v180, v181\n"
"v_smfmac_f32_16x16x64_bf16 v[188:191], v[20:23], a[152:159], v216\n"
"ds_read_b128 v[60:63], v225 offset:3072\n"
"v_cvt_pk_bf16_f32 v243, v182, v183\n"
"v_smfmac_f32_16x16x64_bf16 v[188:191], v[24:27], a[168:175], v216\n"
"ds_read_b128 v[40:43], v225 offset:34816\n"
"v_cvt_pk_bf16_f32 v241, v178, v179\n"
"v_smfmac_f32_16x16x64_bf16 v[188:191], v[28:31], a[184:191], v216\n"
"v_cvt_pk_bf16_f32 v240, v176, v177\n"
"v_smfmac_f32_16x16x64_bf16 v[184:187], v[16:19], a[128:135], v216\n"
"v_mfma_f32_16x16x32_bf16 v[176:179], v[200:203], v[240:243], 0\n"
"v_cvt_pk_bf16_f32 v246, v188, v189\n"
"v_mfma_f32_16x16x32_bf16 v[180:183], v[204:207], v[240:243], 0\n"
"v_cvt_pk_bf16_f32 v247, v190, v191\n"
"v_smfmac_f32_16x16x64_bf16 v[184:187], v[20:23], a[144:151], v216\n"
"ds_read_b128 v[44:47], v225 offset:35840\n"
"v_smfmac_f32_16x16x64_bf16 v[184:187], v[24:27], a[160:167], v216\n"
"ds_read_b128 v[52:55], v225 offset:1024\n"
"s_nop 0\n"
"v_cndmask_b32 v176, v176, v180, s[64:65]\n"
"v_smfmac_f32_16x16x64_bf16 v[184:187], v[28:31], a[176:183], v216\n"
"ds_read_b128 v[32:35], v225 offset:32768\n"
"v_cndmask_b32 v179, v179, v183, s[64:65]\n"
"v_mfma_f32_16x16x16_bf16 v[188:191], v[246:247], v[218:219], 0\n"
"v_cndmask_b32 v177, v177, v181, s[64:65]\n"
"v_cndmask_b32 v178, v178, v182, s[64:65]\n"
"s_nop 1\n"
"v_cvt_pk_bf16_f32 v244, v184, v185\n"
"v_cvt_pk_bf16_f32 v245, v186, v187\n"
"v_smfmac_f32_16x16x64_bf16 v[196:199], v[4:7], a[216:223], v216\n"
"ds_write_b128 v225, v[176:179] offset:38912\n"
"v_mfma_f32_16x16x16_bf16 v[184:187], v[244:245], v[218:219], 0\n"
"v_cvt_pk_bf16_f32 v251, v190, v191\n"
"v_cvt_pk_bf16_f32 v250, v188, v189\n"
"v_smfmac_f32_16x16x64_bf16 v[192:195], v[8:11], a[224:231], v216\n"
"ds_read_b128 v[4:7], v225 offset:9216\n"
"s_nop 3\n"
"v_cvt_pk_bf16_f32 v248, v184, v185\n"
"v_cvt_pk_bf16_f32 v249, v186, v187\n"
"v_smfmac_f32_16x16x64_bf16 v[196:199], v[8:11], a[232:239], v216\n"
"ds_read_b128 v[8:11], v225 offset:10240\n"
"v_mfma_f32_16x16x32_bf16 v[184:187], v[248:251], v[240:243], 0\n"
"s_waitcnt lgkmcnt(2)\n"
"s_barrier\n"
"v_smfmac_f32_16x16x64_bf16 v[196:199], v[12:15], a[248:255], v216\n"
"ds_read_b128 v[180:183], v226 offset:38912\n"
"s_nop 3\n"
"v_cndmask_b32 v185, v185, v187, s[64:65]\n"
"v_smfmac_f32_16x16x64_bf16 v[196:199], v[16:19], v[40:47], v216\n"
"ds_read_b128 v[204:207], v228 offset:38912\n"
"v_cndmask_b32 v184, v184, v186, s[64:65]\n"
"v_smfmac_f32_16x16x64_bf16 v[192:195], v[12:15], a[240:247], v216\n"
"ds_write_b64 v225, v[184:185] offset:39936\n"
"ds_read_b128 v[200:203], v227 offset:38912\n"
"v_smfmac_f32_16x16x64_bf16 v[196:199], v[20:23], v[72:79], v216\n"
"buffer_store_dwordx4 v[248:251], v217, s[48:51], s79 offen sc1\n"
"v_smfmac_f32_16x16x64_bf16 v[192:195], v[16:19], v[32:39], v216\n"
"s_waitcnt lgkmcnt(0)\n"
"v_add_f32 v202, v202, v206\n"
"v_add_f32 v177, v177, v181\n"
"v_smfmac_f32_16x16x64_bf16 v[196:199], v[24:27], v[88:95], v216\n"
"s_barrier\n"
"v_smfmac_f32_16x16x64_bf16 v[196:199], v[28:31], v[104:111], v216\n"
"ds_read_b64 v[190:191], v228 offset:39936\n"
"v_add_f32 v200, v200, v204\n"
"v_smfmac_f32_16x16x64_bf16 v[192:195], v[20:23], v[64:71], v216\n"
"ds_read_b64 v[186:187], v226 offset:39936\n"
"v_add_f32 v201, v201, v205\n"
"v_add_f32 v203, v203, v207\n"
"v_smfmac_f32_16x16x64_bf16 v[192:195], v[24:27], v[80:87], v216\n"
"ds_read_b64 v[188:189], v227 offset:39936\n"
"v_add_f32 v176, v176, v180\n"
"v_add_f32 v178, v178, v182\n"
"v_add_f32 v176, v176, v200\n"
"v_add_f32 v179, v179, v183\n"
"v_add_f32 v178, v178, v202\n"
"v_add_f32 v177, v177, v201\n"
"v_add_f32 v179, v179, v203\n"
"v_max3_f32 v230, v176, v177, v178\n"
"v_cvt_pk_bf16_f32 v247, v198, v199\n"
"v_max_f32 v230, v230, v179\n"
"v_mov_b32 v229, v230\n"
"v_smfmac_f32_16x16x64_bf16 v[192:195], v[28:31], v[96:103], v216\n"
"ds_read_b128 v[12:15], v225 offset:11264\n"
"s_waitcnt lgkmcnt(1)\n"
"v_add_f32 v188, v188, v190\n"
"v_add_f32 v185, v185, v187\n"
"v_permlane32_swap_b32 v230, v229\n"
"v_add_f32 v184, v184, v186\n"
"v_add_f32 v189, v189, v191\n"
"v_max_f32 v230, v230, v229\n"
"v_add_f32 v184, v184, v188\n"
"v_cvt_pk_bf16_f32 v245, v194, v195\n"
"v_add_f32 v185, v185, v189\n"
"v_max3_f32 v230, v230, v184, v185\n"
"v_cvt_pk_bf16_f32 v246, v196, v197\n"
"v_cvt_pk_bf16_f32 v244, v192, v193\n"
"v_max_f32_dpp v230, v230, v230 row_ror:8 row_mask:0xf bank_mask:0xf\n"
"v_mov_b32 v229, v230\n"
"v_permlane32_swap_b32 v193, v197\n"
"s_nop 0\n"
"v_permlane16_swap_b32 v230, v229\n"
"v_max_f32 v230, v230, v229\n"
"v_mul_f32 v231, s85, v230\n"
"v_fma_f32 v179, v179, s85, -v231\n"
"v_exp_f32 v179, v179\n"
"v_fma_f32 v178, v178, s85, -v231\n"
"v_fma_f32 v176, v176, s85, -v231\n"
"v_fma_f32 v177, v177, s85, -v231\n"
"v_exp_f32 v178, v178\n"
"v_fma_f32 v185, v185, s85, -v231\n"
"v_exp_f32 v176, v176\n"
"v_permlane32_swap_b32 v194, v198\n"
"v_cvt_pk_bf16_f32 v237, v178, v179\n"
"v_exp_f32 v177, v177\n"
"v_add_f32 v194, v194, v198\n"
"v_fma_f32 v184, v184, s85, -v231\n"
"v_cvt_pk_bf16_f32 v239, v178, v179\n"
"v_cvt_pk_bf16_f32 v238, v176, v177\n"
"v_cvt_pk_bf16_f32 v236, v176, v177\n"
"v_mov_b32_dpp v239, v237 row_ror:8 row_mask:0xf bank_mask:0x3\n"
"v_mov_b32_dpp v237, v237 row_ror:8 row_mask:0xf bank_mask:0xc\n"
"v_mov_b32_dpp v238, v236 row_ror:8 row_mask:0xf bank_mask:0x3\n"
"v_mov_b32_dpp v236, v236 row_ror:8 row_mask:0xf bank_mask:0xc\n"
"v_exp_f32 v184, v184\n"
"v_permlane32_swap_b32 v192, v196\n"
"v_add_f32 v193, v193, v197\n"
"v_exp_f32 v185, v185\n"
"v_mfma_f32_16x16x32_bf16 v[180:183], v[220:223], v[236:239], 0\n"
"v_cvt_pk_bf16_f32 v189, v184, v185\n"
"v_cvt_pk_bf16_f32 v188, v184, v185\n"
"v_mfma_f32_16x16x32_bf16 v[200:203], v[208:211], v[236:239], 0\n"
"v_add_f32 v192, v192, v196\n"
"v_cvt_pk_bf16_f32 v192, v192, v193\n"
"v_mov_b32_dpp v189, v188 row_ror:8 row_mask:0xf bank_mask:0x3\n"
"v_mov_b32_dpp v188, v188 row_ror:8 row_mask:0xf bank_mask:0xc\n"
"v_mfma_f32_16x16x32_bf16 v[204:207], v[212:215], v[236:239], 0\n"
"v_permlane32_swap_b32 v195, v199\n"
"v_mfma_f32_16x16x16_bf16 v[176:179], v[220:221], v[188:189], 0\n"
"v_add_f32 v195, v195, v199\n"
"v_cvt_pk_bf16_f32 v193, v194, v195\n"
"s_nop 1\n"
"v_mfma_f32_16x16x16_bf16 v[204:207], v[246:247], v[188:189], v[204:207]\n"
"buffer_store_dwordx2 v[192:193], v255, s[48:51], s83 offen offset:1024 sc1\n"
"v_mfma_f32_16x16x16_bf16 v[200:203], v[244:245], v[188:189], v[200:203]\n"
"s_nop 0\n"
"v_fma_f32 v230, v176, 0.5, v180\n"
"v_rcp_f32 v230, v230\n"
"s_nop 2\n"
"v_pk_mul_f32 v[206:207], v[206:207], v[230:231] op_sel_hi:[1,0]\n"
"v_pk_mul_f32 v[204:205], v[204:205], v[230:231] op_sel_hi:[1,0]\n"
"v_cvt_pk_bf16_f32 v242, v204, v205\n"
"v_pk_mul_f32 v[200:201], v[200:201], v[230:231] op_sel_hi:[1,0]\n"
"v_pk_mul_f32 v[202:203], v[202:203], v[230:231] op_sel_hi:[1,0]\n"
"v_cvt_pk_bf16_f32 v241, v202, v203\n"
"v_cvt_pk_bf16_f32 v240, v200, v201\n"
"v_cvt_pk_bf16_f32 v243, v206, v207\n"
"ds_write_b128 v225, v[240:243] offset:38912\n"
"buffer_load_dwordx2 v[234:235], off, s[40:43], s87 sc1\n"
"v_mov_b64 v[178:179], 0\n"
"v_mov_b64 v[176:177], 0\n"
"v_mov_b64 v[182:183], 0\n"
"v_mov_b64 v[184:185], 0\n"
"v_mov_b64 v[180:181], 0\n"
"v_smfmac_f32_16x16x64_bf16 v[176:179], v[240:243], v[112:119], v216\n"
"ds_read_b128 v[40:43], v225 offset:6144\n"
"v_mov_b64 v[188:189], 0\n"
"v_mov_b64 v[186:187], 0\n"
"s_waitcnt lgkmcnt(1)\n"
"s_barrier\n"
"ds_read_b128 v[212:215], v228 offset:38912\n"
"v_smfmac_f32_16x16x64_bf16 v[184:187], v[240:243], v[144:151], v216\n"
"s_waitcnt vmcnt(3)\n"
"ds_read_b128 v[252:255], v226 offset:38912\n"
"ds_read_b128 v[208:211], v227 offset:38912\n"
"v_mov_b64 v[190:191], 0\n"
"v_smfmac_f32_16x16x64_bf16 v[180:183], v[240:243], v[128:135], v216\n"
"ds_read_b128 v[28:31], v225 offset:15360\n"
"v_smfmac_f32_16x16x64_bf16 v[188:191], v[240:243], v[160:167], v216\n"
"ds_read_b128 v[20:23], v225 offset:13312\n"
"s_waitcnt lgkmcnt(4)\n"
"v_smfmac_f32_16x16x64_bf16 v[176:179], v[212:215], v[120:127], v216\n"
"ds_read_b128 v[16:19], v225 offset:12288\n"
"s_waitcnt lgkmcnt(4)\n"
"v_smfmac_f32_16x16x64_bf16 v[176:179], v[252:255], v[48:55], v216\n"
"ds_read_b128 v[32:35], v225 offset:4096\n"
"s_waitcnt lgkmcnt(4)\n"
"v_smfmac_f32_16x16x64_bf16 v[176:179], v[208:211], v[0:7], v216\n"
"ds_read_b128 v[44:47], v225 offset:7168\n"
"v_smfmac_f32_16x16x64_bf16 v[180:183], v[212:215], v[136:143], v216\n"
"ds_read_b128 v[24:27], v225 offset:14336\n"
"v_smfmac_f32_16x16x64_bf16 v[180:183], v[252:255], v[56:63], v216\n"
"ds_read_b128 v[36:39], v225 offset:5120\n"
"v_cvt_pk_bf16_f32 v176, v176, v177\n"
"v_smfmac_f32_16x16x64_bf16 v[180:183], v[208:211], v[8:15], v216\n"
"v_cvt_pk_bf16_f32 v177, v178, v179\n"
"v_smfmac_f32_16x16x64_bf16 v[188:191], v[212:215], v[168:175], v216\n"
"v_min_u32 v176, -2, v176\n"
"v_smfmac_f32_16x16x64_bf16 v[184:187], v[212:215], v[152:159], v216\n"
"s_waitcnt lgkmcnt(2)\n"
"v_smfmac_f32_16x16x64_bf16 v[188:191], v[252:255], v[40:47], v216\n"
"v_cvt_pk_bf16_f32 v178, v180, v181\n"
"s_waitcnt lgkmcnt(0)\n"
"v_smfmac_f32_16x16x64_bf16 v[184:187], v[252:255], v[32:39], v216\n"
"v_min_u32 v178, -2, v178\n"
"v_smfmac_f32_16x16x64_bf16 v[184:187], v[208:211], v[16:23], v216\n"
"v_cvt_pk_bf16_f32 v179, v182, v183\n"
"v_smfmac_f32_16x16x64_bf16 v[188:191], v[208:211], v[24:31], v216\n"
"buffer_store_dwordx4 v[176:179], v224, s[40:43], s72 offen sc1\n"
"s_nop 2\n"
"v_cvt_pk_bf16_f32 v184, v184, v185\n"
"v_cvt_pk_bf16_f32 v185, v186, v187\n"
"s_nop 1\n"
"v_cvt_pk_bf16_f32 v186, v188, v189\n"
"v_cvt_pk_bf16_f32 v187, v190, v191\n"
"v_min_u32 v184, -2, v184\n"
"v_min_u32 v186, -2, v186\n"
"buffer_store_dwordx4 v[184:187], v224, s[40:43], s74 offen sc1\n"
"s_memrealtime s[96:97]\n"
"s_add_u32 s66, s66, 1\n"
"v_readlane_b32 s70, v233, 0\n"
"v_readlane_b32 s80, v233, 1\n"
"s_nop 3\n"
"s_min_u32 s70, s70, s80\n"
"s_waitcnt vmcnt(2)\n"
"v_readfirstlane_b32 s80, v235\n"
"v_mov_b32 v229, s66\n"
"s_waitcnt vmcnt(2)\n"
"buffer_store_dword v229, off, s[32:35], s71 sc1\n"
"s_waitcnt lgkmcnt(0)\n"
"s_sub_u32 s96, s96, s86\n"
"s_min_u32 s96, s96, 170\n"
"s_max_u32 s61, s96, s76\n"
"s_mov_b32 s76, s96\n"
"s_cmp_eq_u32 s80, -1\n"
"s_cselect_b32 s86, s77, s80\n"
"s_xor_b32 s69, s69, 524288\n"
"KVCHK%=:\n"
"s_cmp_lt_u32 s66, s70\n"
"s_cbranch_scc0 KVPOLL%=\n"
"s_add_u32 s79, s66, s59\n"
"s_and_b32 s79, s79, 63\n"
"s_lshl_b32 s78, s79, 14\n"
"s_add_u32 s78, s78, s91\n"
"buffer_load_dwordx4 v[200:203], v217, s[44:47], s78 offen offset:0 sc1\n"
"buffer_load_dwordx4 v[204:207], v217, s[44:47], s78 offen offset:1024 sc1\n"
"buffer_load_dwordx4 v[208:211], v217, s[44:47], s78 offen offset:2048 sc1\n"
"buffer_load_dwordx4 v[212:215], v217, s[44:47], s78 offen offset:3072 sc1\n"
"ds_read_b128 v[32:35], v225 offset:16384\n"
"ds_read_b128 v[36:39], v225 offset:17408\n"
"ds_read_b128 v[40:43], v225 offset:18432\n"
"ds_read_b128 v[44:47], v225 offset:19456\n"
"ds_read_b128 v[48:51], v225 offset:20480\n"
"ds_read_b128 v[52:55], v225 offset:21504\n"
"ds_read_b128 v[56:59], v225 offset:22528\n"
"ds_read_b128 v[60:63], v225 offset:23552\n"
"s_lshl_b32 s79, s79, 13\n"
"s_add_u32 s79, s79, s92\n"
"s_and_b32 s83, s56, 1\n"
"s_mul_i32 s83, s83, 1536\n"
"s_sub_u32 s83, s79, s83\n"
"s_and_b32 s80, s66, 3\n"
"s_lshl_b32 s80, s80, 14\n"
"s_lshl_b32 s68, s56, 11\n"
"s_add_u32 s68, s68, s80\n"
"s_add_u32 s68, s68, 1048576\n"
"s_add_u32 s72, s69, s90\n"
"s_add_u32 s73, s72, 32768\n"
"s_add_u32 s74, s72, 65536\n"
"s_add_u32 s75, s72, 98304\n"
"v_mov_b64 v[176:177], 0\n"
"v_mov_b64 v[178:179], 0\n"
"v_mov_b64 v[180:181], 0\n"
"v_mov_b64 v[182:183], 0\n"
"v_mov_b64 v[184:185], 0\n"
"v_mov_b64 v[186:187], 0\n"
"v_mov_b64 v[188:189], 0\n"
"v_mov_b64 v[190:191], 0\n"
"v_mov_b64 v[192:193], 0\n"
"v_mov_b64 v[194:195], 0\n"
"v_mov_b64 v[196:197], 0\n"
"v_mov_b64 v[198:199], 0\n"
"v_lshrrev_b32 v255, 1, v217\n"
"v_lshlrev_b32 v253, 2, v217\n"
"s_add_u32 s89, s66, 1\n"
"s_add_u32 s88, s89, s59\n"
"s_and_b32 s88, s88, 63\n"
"s_lshl_b32 s88, s88, 14\n"
"s_add_u32 s88, s88, s91\n"
"s_cmp_lt_u32 s89, s70\n"
"s_cselect_b32 s88, s88, s78\n"
"s_memrealtime s[94:95]\n"
"s_waitcnt lgkmcnt(0)\n"
"s_sub_i32 s80, s86, s94\n"
"s_cmp_lt_i32 s80, 0\n"
"s_cselect_b32 s95, s60, 0\n"
"s_cselect_b32 s86, s94, s86\n"
"s_addc_u32 s93, s93, 0\n"
"s_add_u32 s86, s86, s95\n"
"s_max_i32 s80, s80, 0\n"
"s_min_i32 s80, s80, 150\n"
"s_mul_i32 s80, s80, 55\n"
"s_lshr_b32 s80, s80, 6\n"
".p2align 5\n"
"DL2_%=:\n"
"s_sub_u32 s80, s80, 1\n"
"s_cbranch_scc0 DL2_%=\n"
"s_branch LOOP%=\n"
"RETRYG%=:\n"
"s_add_u32 s84, s84, 1\n"
"s_sub_u32 s67, s67, 1\n"
"s_cbranch_scc1 DONE%=\n"
"s_cmp_lg_u32 s81, s66\n"
"s_cselect_b32 s80, 0, 0\n"
"s_add_u32 s57, s57, s80\n"
"s_add_u32 s58, s58, s80\n"
"s_mov_b32 s81, s66\n"
"s_memrealtime s[94:95]\n"
"s_waitcnt lgkmcnt(0)\n"
"s_add_u32 s86, s94, s60\n"
"s_branch LOOP%=\n"
"KVPOLL%=:\n"
"s_sub_u32 s67, s67, 1\n"
"s_cbranch_scc1 DONE%=\n"
"s_sleep 2\n"
"buffer_load_dword v233, v232, s[36:39], 0 offen sc1\n"
"s_waitcnt vmcnt(0)\n"
"v_readlane_b32 s70, v233, 0\n"
"v_readlane_b32 s80, v233, 1\n"
"s_nop 3\n"
"s_min_u32 s70, s70, s80\n"
"s_branch KVCHK%=\n"
"LASTB%=:\n"
"ds_write_b128 v225, v[4:7] offset:37888\n"
"v_smfmac_f32_16x16x64_bf16 v[196:199], v[0:3], a[200:207], v216\n"
"ds_read_b128 v[36:39], v225 offset:25600\n"
"ds_write_b128 v225, v[0:3] offset:36864\n"
"v_smfmac_f32_16x16x64_bf16 v[188:191], v[0:3], a[104:111], v216\n"
"ds_read_b128 v[32:35], v225 offset:24576\n"
"v_smfmac_f32_16x16x64_bf16 v[188:191], v[4:7], a[120:127], v216\n"
"ds_read_b128 v[40:43], v225 offset:26624\n"
"ds_read_b128 v[44:47], v225 offset:27648\n"
"v_smfmac_f32_16x16x64_bf16 v[192:195], v[0:3], a[192:199], v216\n"
"ds_read_b128 v[56:59], v225 offset:30720\n"
"v_smfmac_f32_16x16x64_bf16 v[184:187], v[0:3], a[96:103], v216\n"
"s_waitcnt lgkmcnt(4)\n"
"s_barrier\n"
"ds_read_b128 v[8:11], v226 offset:36864\n"
"v_smfmac_f32_16x16x64_bf16 v[184:187], v[4:7], a[112:119], v216\n"
"ds_read_b128 v[12:15], v226 offset:37888\n"
"v_smfmac_f32_16x16x64_bf16 v[196:199], v[4:7], a[216:223], v216\n"
"ds_read_b128 v[16:19], v227 offset:36864\n"
"ds_read_b128 v[52:55], v225 offset:29696\n"
"v_smfmac_f32_16x16x64_bf16 v[192:195], v[4:7], a[208:215], v216\n"
"ds_read_b128 v[60:63], v225 offset:31744\n"
"ds_read_b128 v[24:27], v228 offset:36864\n"
"s_waitcnt lgkmcnt(5)\n"
"v_smfmac_f32_16x16x64_bf16 v[188:191], v[8:11], v[40:47], v216\n"
"ds_read_b128 v[40:43], v225 offset:34816\n"
"v_smfmac_f32_16x16x64_bf16 v[196:199], v[8:11], a[232:239], v216\n"
"ds_read_b128 v[20:23], v227 offset:37888\n"
"v_smfmac_f32_16x16x64_bf16 v[184:187], v[8:11], v[32:39], v216\n"
"ds_read_b128 v[36:39], v225 offset:33792\n"
"v_smfmac_f32_16x16x64_bf16 v[192:195], v[8:11], a[224:231], v216\n"
"ds_read_b128 v[32:35], v225 offset:32768\n"
"s_waitcnt lgkmcnt(5)\n"
"v_smfmac_f32_16x16x64_bf16 v[188:191], v[12:15], v[56:63], v216\n"
"v_smfmac_f32_16x16x64_bf16 v[188:191], v[16:19], a[136:143], v216\n"
"ds_read_b128 v[48:51], v225 offset:28672\n"
"v_smfmac_f32_16x16x64_bf16 v[192:195], v[12:15], a[240:247], v216\n"
"s_waitcnt lgkmcnt(3)\n"
"v_smfmac_f32_16x16x64_bf16 v[188:191], v[20:23], a[152:159], v216\n"
"v_smfmac_f32_16x16x64_bf16 v[196:199], v[12:15], a[248:255], v216\n"
"ds_read_b128 v[44:47], v225 offset:35840\n"
"v_smfmac_f32_16x16x64_bf16 v[188:191], v[24:27], a[168:175], v216\n"
"ds_read_b128 v[28:31], v228 offset:37888\n"
"s_waitcnt lgkmcnt(3)\n"
"v_smfmac_f32_16x16x64_bf16 v[192:195], v[16:19], v[32:39], v216\n"
"v_smfmac_f32_16x16x64_bf16 v[192:195], v[20:23], v[64:71], v216\n"
"v_smfmac_f32_16x16x64_bf16 v[192:195], v[24:27], v[80:87], v216\n"
"s_waitcnt lgkmcnt(1)\n"
"v_smfmac_f32_16x16x64_bf16 v[196:199], v[16:19], v[40:47], v216\n"
"s_waitcnt lgkmcnt(0)\n"
"v_smfmac_f32_16x16x64_bf16 v[188:191], v[28:31], a[184:191], v216\n"
"v_smfmac_f32_16x16x64_bf16 v[196:199], v[20:23], v[72:79], v216\n"
"v_smfmac_f32_16x16x64_bf16 v[196:199], v[24:27], v[88:95], v216\n"
"v_smfmac_f32_16x16x64_bf16 v[192:195], v[28:31], v[96:103], v216\n"
"v_cvt_pk_bf16_f32 v246, v188, v189\n"
"v_smfmac_f32_16x16x64_bf16 v[196:199], v[28:31], v[104:111], v216\n"
"v_smfmac_f32_16x16x64_bf16 v[184:187], v[12:15], v[48:55], v216\n"
"v_cvt_pk_bf16_f32 v247, v190, v191\n"
"v_smfmac_f32_16x16x64_bf16 v[184:187], v[16:19], a[128:135], v216\n"
"v_permlane32_swap_b32 v193, v197\n"
"v_smfmac_f32_16x16x64_bf16 v[184:187], v[20:23], a[144:151], v216\n"
"v_permlane32_swap_b32 v195, v199\n"
"v_add_f32 v193, v193, v197\n"
"v_smfmac_f32_16x16x64_bf16 v[184:187], v[24:27], a[160:167], v216\n"
"v_permlane32_swap_b32 v192, v196\n"
"v_smfmac_f32_16x16x64_bf16 v[184:187], v[28:31], a[176:183], v216\n"
"v_permlane32_swap_b32 v194, v198\n"
"v_add_f32 v194, v194, v198\n"
"v_add_f32 v192, v192, v196\n"
"v_mfma_f32_16x16x16_bf16 v[188:191], v[246:247], v[218:219], 0\n"
"s_nop 3\n"
"v_cvt_pk_bf16_f32 v244, v184, v185\n"
"v_cvt_pk_bf16_f32 v245, v186, v187\n"
"v_cvt_pk_bf16_f32 v192, v192, v193\n"
"v_add_f32 v195, v195, v199\n"
"v_cvt_pk_bf16_f32 v193, v194, v195\n"
"v_mfma_f32_16x16x16_bf16 v[184:187], v[244:245], v[218:219], 0\n"
"buffer_store_dwordx2 v[192:193], v255, s[48:51], s83 offen offset:1024 sc1\n"
"v_cvt_pk_bf16_f32 v250, v188, v189\n"
"v_cvt_pk_bf16_f32 v251, v190, v191\n"
"s_nop 4\n"
"v_cvt_pk_bf16_f32 v249, v186, v187\n"
"v_cvt_pk_bf16_f32 v248, v184, v185\n"
"buffer_store_dwordx4 v[248:251], v217, s[48:51], s79 offen sc1\n"
"v_mov_b32 v230, 64\n"
"s_waitcnt vmcnt(0)\n"
"buffer_store_dword v230, off, s[32:35], s71 sc1\n"
"DONE%=:\n"
"s_waitcnt vmcnt(0) lgkmcnt(0)\n"
        : "={s[40:43]}"(oR), "={s57}"(o1), "={s58}"(o2), "={s61}"(o3), "={v[230:233]}"(oV)
        : "0"(rR), "1"(p.d1), "2"(p.d2), "3"(p.d0), "4"(pa), "{s[52:53]}"(p.Wq + h * 128), "{s[54:55]}"(p.Wk + h * 128), "{s[12:13]}"(p.Wv + h * 128), "{s[14:15]}"(p.Wo + h * 65536), "{s[44:47]}"(rKV), "{s[48:51]}"(rM), "{s[36:39]}"(rF), "{s[32:35]}"(rG), "{s[28:31]}"(rD), "{s56}"(w), "{s59}"(h), "{s60}"(p.mg),
          "{v216}"((n & 8) ? 0xEEEE : 0x4444), "{v217}"(lane * 16), "{v[218:219]}"(sel), "{v[220:223]}"(ones), "{v224}"(g * 16384 + n * 16 + h * 256),
          "{v225}"(lb + w * 40960), "{v226}"(lb + ((w + 1) & 3) * 40960), "{v227}"(lb + ((w + 2) & 3) * 40960), "{v228}"(lb + ((w + 3) & 3) * 40960)
        : "v0","v1","v2","v3","v4","v5","v6","v7","v8","v9","v10","v11","v12","v13","v14","v15","v16","v17","v18","v19","v20","v21","v22","v23","v24","v25","v26","v27","v28","v29","v30","v31","v32","v33","v34","v35","v36","v37","v38","v39","v40","v41","v42","v43","v44","v45","v46","v47","v48","v49","v50","v51","v52","v53","v54","v55","v56","v57","v58","v59","v60","v61","v62","v63","v64","v65","v66","v67","v68","v69","v70","v71","v72","v73","v74","v75","v76","v77","v78","v79","v80","v81","v82","v83","v84","v85","v86","v87","v88","v89","v90","v91","v92","v93","v94","v95","v96","v97","v98","v99","v100","v101","v102","v103","v104","v105","v106","v107","v108","v109","v110","v111","v112","v113","v114","v115","v116","v117","v118","v119","v120","v121","v122","v123","v124","v125","v126","v127","v128","v129","v130","v131","v132","v133","v134","v135","v136","v137","v138","v139","v140","v141","v142","v143","v144","v145","v146","v147","v148","v149","v150","v151","v152","v153","v154","v155","v156","v157","v158","v159","v160","v161","v162","v163","v164","v165","v166","v167","v168","v169","v170","v171","v172","v173","v174","v175","v176","v177","v178","v179","v180","v181","v182","v183","v184","v185","v186","v187","v188","v189","v190","v191","v192","v193","v194","v195","v196","v197","v198","v199","v200","v201","v202","v203","v204","v205","v206","v207","v208","v209","v210","v211","v212","v213","v214","v215","v229","v234","v235","v236","v237","v238","v239","v240","v241","v242","v243","v244","v245","v246","v247","v248","v249","v250","v251","v252","v253","v254","v255","a0","a1","a2","a3","a4","a5","a6","a7","a8","a9","a10","a11","a12","a13","a14","a15","a16","a17","a18","a19","a20","a21","a22","a23","a24","a25","a26","a27","a28","a29","a30","a31","a32","a33","a34","a35","a36","a37","a38","a39","a40","a41","a42","a43","a44","a45","a46","a47","a48","a49","a50","a51","a52","a53","a54","a55","a56","a57","a58","a59","a60","a61","a62","a63","a64","a65","a66","a67","a68","a69","a70","a71","a72","a73","a74","a75","a76","a77","a78","a79","a80","a81","a82","a83","a84","a85","a86","a87","a88","a89","a90","a91","a92","a93","a94","a95","a96","a97","a98","a99","a100","a101","a102","a103","a104","a105","a106","a107","a108","a109","a110","a111","a112","a113","a114","a115","a116","a117","a118","a119","a120","a121","a122","a123","a124","a125","a126","a127","a128","a129","a130","a131","a132","a133","a134","a135","a136","a137","a138","a139","a140","a141","a142","a143","a144","a145","a146","a147","a148","a149","a150","a151","a152","a153","a154","a155","a156","a157","a158","a159","a160","a161","a162","a163","a164","a165","a166","a167","a168","a169","a170","a171","a172","a173","a174","a175","a176","a177","a178","a179","a180","a181","a182","a183","a184","a185","a186","a187","a188","a189","a190","a191","a192","a193","a194","a195","a196","a197","a198","a199","a200","a201","a202","a203","a204","a205","a206","a207","a208","a209","a210","a211","a212","a213","a214","a215","a216","a217","a218","a219","a220","a221","a222","a223","a224","a225","a226","a227","a228","a229","a230","a231","a232","a233","a234","a235","a236","a237","a238","a239","a240","a241","a242","a243","a244","a245","a246","a247","a248","a249","a250","a251","a252","a253","a254","a255","s4","s5","s6","s7","s8","s9","s10","s11","s16","s17","s18","s19","s20","s21","s22","s23","s24","s25","s26","s27","s62","s63","s64","s65","s66","s67","s68","s69","s70","s71","s72","s73","s74","s75","s76","s77","s78","s79","s80","s81","s82","s83","s84","s85","s86","s87","s88","s89","s90","s91","s92","s93","s94","s95","s96","s97","s98","s99","vcc","scc","memory");
}
__global__ __launch_bounds__(256, 1) void k_main(Par p) {
    extern __shared__ __attribute__((aligned(16))) char lds[];
    const int bx = blockIdx.x, ia = (bx >> 3) * 2 + (bx & 1), ib = (bx >> 3) * 4 + (bx & 3) - 96;
    const int bi = p.e0 & 16 ? bx : (bx & 6) == 2 && bx < 128 ? (24 + (ia >> 2)) * 8 + 4 + (ia & 3) : (bx & 4) && ib >= 0 ? (ib >> 1) * 8 + 2 + (ib & 1) : bx;
    const int x = bi & 7, r = bi >> 3, lane = threadIdx.x & 63, w = __builtin_amdgcn_readfirstlane(threadIdx.x >> 6);
    rsrc_t rF = mkr(p.ws + O_FLG, F_N * 128); int bud = 200000;
    if (w == 3 && bx >= 128 && !(p.e1 & 512)) touch(code_span(), lane * 128 + ((bx >> 3) - 16) * 8192, 0, (unsigned)(size_t)lds + 159 * 1024);
    if (x < 4) {
        if (x < 2) { if (r < 16) { recur(fresh(), lds, r * 2 + x, w, lane); return; } else if (w == 0) reducer(p, r * 2 + x - 32, lane, rF, bud);
            else if (!(p.e0 & 64) && bi != 128) { for (const unsigned t = rt(); rt() - t < (unsigned)p.e0 >> 8;) __builtin_amdgcn_s_sleep(8);
                const Par p = fresh(); const int h = r * 2 + x - 32;
                for (int i = (p.e1 >> 16) + w - 1; i < 48; i += 3) { if (i < 32) touch(mkd((i < 16 ? p.Wk : p.Wv) + h * 128, 511 * 8192 + 256), (lane >> 1) * 8192 + (lane & 1) * 128, ((i & 3) * 4 + ((i >> 2) & 3)) << 18, (unsigned)(size_t)lds);
                    else touch(mkd(p.Wo + h * 65536, 131072), lane * 128, (i - 32) * 8192, (unsigned)(size_t)lds); }
                drain(); } }
        else { const int L = r * 2 + x - 2; const Par p = fresh();

            ln1(p, lds, L, w, lane, rF); STAMP(0)
            if (L < 32 && !(p.e0 & 32)) warm(p, lds, L * 4 + w, 1282, 128, lane);
            wtile(p, L, w, lane, rF);
            if (L < 32) { if (!(p.e0 & 2)) redx(p, lds, L, w, lane, rF, bud); } else if (L < 54 && !(p.e0 & 4)) win(p, lds, L - 32, w, lane, rF, bud); else if (L >= 58) wotile(p, lds, L - 58, w, lane); }
    } else {
        const int H = r * 4 + x - 4, h = H & 31, q = H >> 5;
        if (q == 3) { if (!(p.e0 & 1)) att(p, lds, h, w, lane, rF, bud); }
        else {
            const int mat = q == 2 ? 0 : q + 1, n = lane & 15, g = lane >> 4; v4u W[16][2];
            wdma<8192>((mat == 0 ? p.Wq : mat == 1 ? p.Wk : p.Wv) + h * 128, 511 * 8192 + 256, w << 20, (unsigned)(size_t)lds + w * 32768, lane);
            drain(); bar(); STAMP(0)
            UNR for (int t = 0; t < 16; ++t) UNR for (int u = 0; u < 2; ++u) W[t][u] = trd(lds + (16 * g + (n >> 2)) * 256 + (w ^ (n >> 2)) * 64 + (n & 3) * 16 + (t >> 1) * 16384 + (t & 1) * 2048 + 8 * u);
            if (mat == 2) qkv<true>(p, lds, W, mat, h, w, lane, rF, bud); else qkv<false>(p, lds, W, mat, h, w, lane, rF, bud);
            if (!(p.e0 & 8)) spat(p, lds, H, w, lane, rF, bud);
        }
    }
}
__global__ __launch_bounds__(256, 1) void k_out(Par p) {
    __shared__ f32x4 red[4][16][64];
    const int b = blockIdx.x, rbk = (b & 7) * 4 + ((b >> 3) & 3), cb = b >> 5, lane = threadIdx.x & 63, w = __builtin_amdgcn_readfirstlane(threadIdx.x >> 6), n = lane & 15, g = lane >> 4;
    rsrc_t rU = mkr(p.ws + O_UF, 3 * MB), rW = mkr(p.ws + O_WOUT, MB); const unsigned l5 = w < 2 ? lane * 16 : OOB;
    v4u U[6][4], W[6][4], x1[2], gb[2]; f32x4 a[4][4] = {};
    UNR for (int i = 0; i < 6; ++i) UNR for (int q = 0; q < 4; ++q) { U[i][q] = ld16(rU, i < 5 ? lane * 16 : l5, ((rbk * 4 + q) * 22 + w + 4 * i) * 1024); W[i][q] = ld16(rW, i < 5 ? lane * 16 : l5, ((cb * 4 + q) * 22 + w + 4 * i) * 1024); }
    UNR for (int q = 0; q < 2; ++q) { x1[q] = *(const v4u*)(p.ws + O_X1 + ((rbk * 4 + w) * 16 + cb * 2 + q) * 1024 + lane * 16); gb[q] = *(const v4u*)(p.ws + O_MOD + (2560 + cb * 64 + q * 32 + 8 * g) * 2); }
    SB();
    UNR for (int i = 0; i < 6; ++i) UNR for (int f = 0; f < 4; ++f) UNR for (int c = 0; c < 4; ++c) a[f][c] = mf32(W[i][c], U[i][f], a[f][c]);
    UNR for (int i = 0; i < 16; ++i) red[w][i][lane] = a[i >> 2][i & 3];
    __syncthreads();
    UNR for (int q = 0; q < 2; ++q) { f32x4 y[2]; v4u o;
        UNR for (int u = 0; u < 2; ++u) { const int i = w * 4 + 2 * q + u; y[u] = (red[0][i][lane] + red[1][i][lane]) + (red[2][i][lane] + red[3][i][lane]); }
        const v4u t = pk8(y[0], y[1]);
        UNR for (int k = 0; k < 4; ++k) { const unsigned m = pk(lo(gb[q][k]) * lo(t[k]), hi(gb[q][k]) * hi(t[k])); o[k] = pk(lo(x1[q][k]) + lo(m), hi(x1[q][k]) + hi(m)); }
        *(v4u*)(p.out + (rbk * 64 + (w >> 1) * 32 + prow(n, w & 1)) * 512 + cb * 64 + q * 32 + 8 * g) = o; }
}
static const int g_par[6] = {P_D1, P_D2, P_MG, P_D0, 128000, 30};
extern "C" void kernel_launch(void* const* d_in, const int* in_sizes, int n_in, void* d_out, int out_size, void* d_ws, size_t ws_size, hipStream_t stream) {
    Par p;
    p.x = (const u16*)d_in[0]; p.mem0 = (const u16*)d_in[1]; p.Wq = (const u16*)d_in[2]; p.Wk = (const u16*)d_in[3]; p.Wv = (const u16*)d_in[4]; p.Wo = (const u16*)d_in[5];
    p.win = (const u16*)d_in[8]; p.wsp = (const u16*)d_in[9]; p.bsp = (const u16*)d_in[10]; p.wout = (const u16*)d_in[11]; p.c = (const u16*)d_in[12]; p.wmod = (const u16*)d_in[13];
    p.ws = (char*)(((size_t)d_ws + 2 * MB - 1) & ~(2 * MB - 1)); p.out = (u16*)d_out; p.d1 = g_par[0]; p.d2 = g_par[1]; p.mg = g_par[2]; p.d0 = g_par[3]; p.e0 = g_par[4]; p.e1 = g_par[5];
    (void)hipFuncSetAttribute((const void*)k_main, hipFuncAttributeMaxDynamicSharedMemorySize, 160 * 1024);
    k_pre<<<PRE_GRID, 256, 0, stream>>>(p);
    k_main<<<256, 256, 160 * 1024, stream>>>(p);
    k_out<<<256, 256, 0, stream>>>(p);
}
```
